```python
import torch

def _softmax(x, axis=-1):
    return torch.softmax(x, dim=axis)
def _gelu(x):
    return torch.nn.functional.gelu(x, approximate="tanh")
def _silu(x):
    return torch.nn.functional.silu(x)
def _sigmoid(x):
    return torch.sigmoid(x)
def _layernorm(x, axis=-1, eps=1e-5):
    m = x.mean(dim=axis, keepdim=True)
    v = x.var(dim=axis, keepdim=True, unbiased=False)
    return (x - m) / torch.sqrt(v + eps)
def _scatter_add(tgt, idx, src, axis=0):
    return tgt.clone().index_add_(axis, idx, src)
def _segment_sum(src, seg, n):
    out = torch.zeros((n,) + src.shape[1:], dtype=src.dtype)
    return out.index_add_(0, seg, src)
def _pad(x, pw):
    flat = tuple(v for pair in reversed(pw) for v in pair)
    return torch.nn.functional.pad(x, flat)

kernel_name = 'sequential_preLN__sliding_local_dilated__glu_gated_geglu__prompt'

def setup_inputs():

    _w = (lambda a: (lambda t: t.to(torch.bfloat16) if t.is_floating_point() else t)(torch.as_tensor(a)))
    def _A_setup():
        B, S, D = 1, 2048, 512
        return {
            "x": _w((lambda *s: torch.randn(*s, dtype=torch.bfloat16) * 0.05)(B, S, D)),
            "wqkv": _w((lambda *s: torch.randn(*s, dtype=torch.bfloat16) * 0.05)(D, 3 * D)),
            "wo": _w((lambda *s: torch.randn(*s, dtype=torch.bfloat16) * 0.05)(D, D)),
        }
    _A_in = _A_setup()

    def _B_setup():
        B, S, Dm, Df = 1, 2048, 512, 1792
        return {
            "x": _w((lambda *s: torch.randn(*s, dtype=torch.bfloat16) * 0.05)(B, S, Dm)),
            "w_gate": _w((lambda *s: torch.randn(*s, dtype=torch.bfloat16) * 0.8)(Dm, Df) * (Dm ** -0.5)),
            "w_up": _w((lambda *s: torch.randn(*s, dtype=torch.bfloat16) * 0.8)(Dm, Df) * (Dm ** -0.5)),
            "w_down": _w((lambda *s: torch.randn(*s, dtype=torch.bfloat16) * 0.8)(Df, Dm) * (Df ** -0.5)),
        }
    _B_in = _B_setup()

    return {
        "A_x": _A_in["x"],
        "A_wqkv": _A_in["wqkv"],
        "A_wo": _A_in["wo"],
        "B_x": _B_in["x"],
        "B_w_gate": _B_in["w_gate"],
        "B_w_up": _B_in["w_up"],
        "B_w_down": _B_in["w_down"],
    }

def reference(A_x, A_wqkv, A_wo, B_x, B_w_gate, B_w_up, B_w_down):
    def _A_ref(x, wqkv, wo):
        B, S, D = x.shape
        H = 32; Dh = D // H
        W, dil = 8, 2
        qkv = (lambda x, *d: x.permute(*d))((x @ wqkv).reshape(B, S, 3, H, Dh), 2, 0, 3, 1, 4)
        q, k, v = qkv[0], qkv[1], qkv[2]
        scale = Dh ** -0.5
        pos = torch.arange(S)
        off = torch.arange(W) * dil
        idx = pos[:, None] - off[None, :]
        mask = idx >= 0
        idx_c = torch.where(mask, idx, 0).reshape(-1)
        kg = k[:, :, idx_c, :].reshape(B, H, S, W, Dh)
        vg = v[:, :, idx_c, :].reshape(B, H, S, W, Dh)
        logits = torch.einsum("bhsd,bhswd->bhsw", q, kg) * scale
        logits = torch.where(mask[None, None, :, :], logits, -1e9)
        attn = _softmax(logits, axis=-1)
        o = torch.einsum("bhsw,bhswd->bhsd", attn, vg)
        return (lambda x, *d: x.permute(*d))(o, 0, 2, 1, 3).reshape(B, S, D) @ wo

    def _B_ref(x, w_gate, w_up, w_down):
        g = torch.einsum("bsd,df->bsf", x, w_gate)
        u = torch.einsum("bsd,df->bsf", x, w_up)
        h = _gelu(g) * u
        return torch.einsum("bsf,fd->bsd", h, w_down)

    x = A_x
    h = x + _A_ref(_layernorm(x, axis=-1), A_wqkv, A_wo)
    h2 = h + _B_ref(_layernorm(h, axis=-1), B_w_gate, B_w_up, B_w_down)
    return h2
```

```cpp
#include <hip/hip_runtime.h>
#include <hip/hip_bf16.h>
typedef unsigned short u16;
using bf16x8 = __attribute__((ext_vector_type(8))) short;
using f32x4  = __attribute__((ext_vector_type(4))) float;
__device__ __forceinline__ float bf2f(u16 b){ return __uint_as_float(((unsigned)b)<<16); }
__device__ __forceinline__ u16 f2bf(float f){ __hip_bfloat16 h = __float2bfloat16(f); return *reinterpret_cast<u16*>(&h); }

constexpr int S=2048, D=512, F=1792;

__global__ void ln_kernel(const u16* x, u16* y){
  int row = blockIdx.x*4 + (threadIdx.x>>6); int lane = threadIdx.x&63;
  const u16* p = x + (size_t)row*D + lane*8;
  float v[8]; float s=0;
  for(int i=0;i<8;i++){ v[i]=bf2f(p[i]); s+=v[i]; }
  for(int m=32;m>=1;m>>=1) s+=__shfl_xor(s,m);
  float mean=s/D; float q=0;
  for(int i=0;i<8;i++){ float d=v[i]-mean; q+=d*d; }
  for(int m=32;m>=1;m>>=1) q+=__shfl_xor(q,m);
  float r = rsqrtf(q/D+1e-5f);
  for(int i=0;i<8;i++) y[(size_t)row*D+lane*8+i]=f2bf((v[i]-mean)*r);
}
template<int MODE>
__global__ void gemm_kernel(const u16* A, const u16* B, const u16* res, u16* C, int M,int N,int K){
  int lane=threadIdx.x&63; int wv=threadIdx.x>>6;
  int bx=(blockIdx.x*4+wv)*16, by=blockIdx.y*16;
  int r=lane&15,q=lane>>4;
  f32x4 acc={0,0,0,0};
  for(int k0=0;k0<K;k0+=32){
    bf16x8 a=*(const bf16x8*)(A+(size_t)(by+r)*K+k0+q*8);
    bf16x8 b;
    for(int j=0;j<8;j++) b[j]=B[(size_t)(k0+q*8+j)*N+bx+r];
    acc=__builtin_amdgcn_mfma_f32_16x16x32_bf16(a,b,acc,0,0,0);
  }
  for(int j=0;j<4;j++){ size_t o=(size_t)(by+q*4+j)*N+bx+r; float v=acc[j]; if(MODE==1) v+=bf2f(res[o]); C[o]=f2bf(v);}
}
__global__ void attn_kernel(const u16* qkv, u16* o){
  int idx=blockIdx.x*blockDim.x+threadIdx.x;
  int s=idx>>5,h=idx&31;
  float q[16]; for(int d=0;d<16;d++) q[d]=bf2f(qkv[(size_t)s*1536+h*16+d]);
  float lg[8]; float mx=-1e30f;
  for(int w=0;w<8;w++){ int t=s-2*w; float a=-1e9f; if(t>=0){ a=0; for(int d=0;d<16;d++) a+=q[d]*bf2f(qkv[(size_t)t*1536+512+h*16+d]); a*=0.25f;} lg[w]=a; mx=fmaxf(mx,a);}
  float sum=0; for(int w=0;w<8;w++){ lg[w]=__expf(lg[w]-mx); sum+=lg[w]; }
  float inv=1.f/sum;
  for(int d=0;d<16;d++){ float a=0; for(int w=0;w<8;w++){ int t=s-2*w; if(t>=0) a+=lg[w]*bf2f(qkv[(size_t)t*1536+1024+h*16+d]); } o[(size_t)s*D+h*16+d]=f2bf(a*inv);}
}
__global__ void geglu_kernel(const u16* g,const u16* u,u16* h,int n){
  int i=blockIdx.x*blockDim.x+threadIdx.x; if(i>=n) return;
  float x=bf2f(g[i]); float t=tanhf(0.7978845608028654f*(x+0.044715f*x*x*x));
  h[i]=f2bf(0.5f*x*(1.f+t)*bf2f(u[i]));
}
extern "C" void kernel_launch(void* const* d_in, const int* in_sizes, int n_in, void* d_out, int out_size, void* d_ws, size_t ws_size, hipStream_t stream){
  const u16* x=(const u16*)d_in[0]; const u16* wqkv=(const u16*)d_in[1]; const u16* wo=(const u16*)d_in[2];
  const u16* wg=(const u16*)d_in[4]; const u16* wu=(const u16*)d_in[5]; const u16* wd=(const u16*)d_in[6];
  u16* ws=(u16*)d_ws;
  u16* xn=ws; u16* qkv=xn+S*D; u16* o=qkv+S*1536; u16* h=o+S*D; u16* hn=h+S*D; u16* g=hn+S*D; u16* u=g+S*F; u16* hm=u+S*F;
  ln_kernel<<<S/4,256,0,stream>>>(x,xn);
  gemm_kernel<0><<<dim3(1536/64,S/16),256,0,stream>>>(xn,wqkv,nullptr,qkv,S,1536,D);
  attn_kernel<<<S*32/256,256,0,stream>>>(qkv,o);
  gemm_kernel<1><<<dim3(D/64,S/16),256,0,stream>>>(o,wo,x,h,S,D,D);
  ln_kernel<<<S/4,256,0,stream>>>(h,hn);
  gemm_kernel<0><<<dim3(F/64,S/16),256,0,stream>>>(hn,wg,nullptr,g,S,F,D);
  gemm_kernel<0><<<dim3(F/64,S/16),256,0,stream>>>(hn,wu,nullptr,u,S,F,D);
  geglu_kernel<<<S*F/256,256,0,stream>>>(g,u,hm,S*F);
  gemm_kernel<1><<<dim3(D/64,S/16),256,0,stream>>>(hm,wd,h,(u16*)d_out,S,D,F);
}
```

```cpp
#include <hip/hip_runtime.h>
#include <stdint.h>

typedef unsigned u32;
typedef unsigned long long u64;
typedef u32 u32x2 __attribute__((ext_vector_type(2)));
typedef u32 u32x4 __attribute__((ext_vector_type(4)));
typedef float f32x2 __attribute__((ext_vector_type(2)));
typedef float f32x4 __attribute__((ext_vector_type(4)));
typedef float f32x16 __attribute__((ext_vector_type(16)));
typedef short s16x4 __attribute__((ext_vector_type(4)));
typedef __bf16 bf16x2 __attribute__((ext_vector_type(2)));
typedef __bf16 bf16x8 __attribute__((ext_vector_type(8)));
#define LDS_AS __attribute__((address_space(3)))
#define GLB_AS __attribute__((address_space(1)))
#define DEVI __device__ __forceinline__

#define DBG_TS 0

template <int N> struct IC { static constexpr int v = N; };
template <int B, int E, class F> DEVI void sfor(F&& f) {
  if constexpr (B < E) { f(IC<B>{}); sfor<B + 1, E>(f); }
}

constexpr u32 WS_EPOCH = 0;
constexpr u32 WS_F1 = 4096;
constexpr u32 WS_F2 = 16384;
constexpr u32 WS_F3 = 32768;
constexpr u32 WS_HSTAT = 65536;
constexpr u32 WS_DBG = 524288;
constexpr u32 WS_OBUF = 1u << 20;
constexpr u32 WS_HBUF = 3u << 20;
constexpr u32 WS_MBUF = 5u << 20;
constexpr u32 WS_SIZE = 12u << 20;

constexpr u32 L_RING = 81920;
constexpr u32 L_SCR = 155648;
constexpr u32 L_TOP = 131072;

DEVI u32x4 lds_r128(u32 a) { return *(LDS_AS u32x4*)(size_t)(a); }
DEVI u32x2 lds_r64(u32 a) { return *(LDS_AS u32x2*)(size_t)(a); }
DEVI u32 lds_r32(u32 a) { return *(LDS_AS u32*)(size_t)(a); }
DEVI void lds_w128(u32 a, u32x4 v) { *(LDS_AS u32x4*)(size_t)(a) = v; }
DEVI void lds_w64(u32 a, u32x2 v) { *(LDS_AS u32x2*)(size_t)(a) = v; }
DEVI void lds_w32(u32 a, u32 v) { *(LDS_AS u32*)(size_t)(a) = v; }
DEVI u32x2 lds_tr(u32 a) {
  return __builtin_bit_cast(u32x2, __builtin_amdgcn_ds_read_tr16_b64_v4i16((LDS_AS s16x4*)(size_t)(a)));
}
DEVI u32x4 cat(u32x2 a, u32x2 b) { return u32x4{a.x, a.y, b.x, b.y}; }
DEVI f32x4 mma16(u32x4 a, u32x4 b, f32x4 c) {
  return __builtin_amdgcn_mfma_f32_16x16x32_bf16(__builtin_bit_cast(bf16x8, a), __builtin_bit_cast(bf16x8, b), c, 0, 0, 0);
}
DEVI f32x4 mma16k16(u32x2 a, u32x2 b, f32x4 c) {
  return __builtin_amdgcn_mfma_f32_16x16x16bf16_1k(__builtin_bit_cast(s16x4, a), __builtin_bit_cast(s16x4, b), c, 0, 0, 0);
}
DEVI f32x16 mma32(u32x4 a, u32x4 b, f32x16 c) {
  return __builtin_amdgcn_mfma_f32_32x32x16_bf16(__builtin_bit_cast(bf16x8, a), __builtin_bit_cast(bf16x8, b), c, 0, 0, 0);
}
DEVI u32 pk2(float a, float b) { bf16x2 v = {(__bf16)a, (__bf16)b}; return __builtin_bit_cast(u32, v); }
DEVI float bflo(u32 v) { return __uint_as_float(v << 16); }
DEVI float bfhi(u32 v) { return __uint_as_float(v & 0xffff0000u); }
DEVI float asf(u32 v) { return __uint_as_float(v); }
DEVI u32 asu(float v) { return __float_as_uint(v); }
#define SB() __builtin_amdgcn_sched_barrier(0)

DEVI void DMA16(u32 ldsaddr, u32 voff, u32x4 srd, u32 soff) {
  asm volatile("s_mov_b32 m0, %0\n\ts_nop 0\n\tbuffer_load_dwordx4 %1, %2, %3 offen lds" ::"s"(ldsaddr), "v"(voff), "s"(srd), "s"(soff) : "memory");
}
DEVI void DMA4(u32 ldsaddr, u32 voff, u32x4 srd, u32 soff) {
  asm volatile("s_mov_b32 m0, %0\n\ts_nop 0\n\tbuffer_load_dword %1, %2, %3 offen lds" ::"s"(ldsaddr), "v"(voff), "s"(srd), "s"(soff) : "memory");
}
template <int OFF> DEVI void DMA16o(u32 ldsaddr, u32 voff, u32x4 srd, u32 soff) {
  asm volatile("s_mov_b32 m0, %0\n\ts_nop 0\n\tbuffer_load_dwordx4 %1, %2, %3 offen offset:%4 lds" ::"s"(ldsaddr), "v"(voff), "s"(srd), "s"(soff), "n"(OFF) : "memory");
}
DEVI void DMA16_TRIPLE(u32 ldsaddr, u32 voff, u32x4 srd, u32 soff) {
  asm volatile("s_mov_b32 m0, %0\n\ts_nop 0\n\tbuffer_load_dwordx4 %1, %2, %3 offen lds\n\t"
               "buffer_load_dwordx4 %1, %2, %3 offen offset:1024 lds\n\t"
               "buffer_load_dwordx4 %1, %2, %3 offen offset:2048 lds" ::"s"(ldsaddr), "v"(voff), "s"(srd), "s"(soff) : "memory");
}
DEVI void MMA32_INIT(f32x16& acc, const u32x4& a, const u32x4& b) {
  asm volatile("v_mfma_f32_32x32x16_bf16 %0, %1, %2, 0" : "=&v"(acc) : "v"(a), "v"(b));
}
DEVI void MMA32_ACC(f32x16& acc, const u32x4& a, const u32x4& b) {
  asm volatile("v_mfma_f32_32x32x16_bf16 %0, %1, %2, %0" : "+v"(acc) : "v"(a), "v"(b));
}
DEVI void MMA32_ACC_A(f32x16& acc, const u32x4& a, const u32x4& b) {
  asm volatile("v_mfma_f32_32x32x16_bf16 %0, %1, %2, %0" : "+v"(acc) : "v"(a), "a"(b));
}
template <class T> DEVI void OPQ(T& v) { asm volatile("" : "+v"(v)); }
#define WAIT_VM(n) asm volatile("s_waitcnt vmcnt(%0)" ::"n"(n) : "memory")
#define WAIT_LGKM0() asm volatile("s_waitcnt lgkmcnt(0)" ::: "memory")
#define BAR() asm volatile("s_waitcnt lgkmcnt(0)\n\ts_barrier" ::: "memory")
#define BAR_VM(n) asm volatile("s_waitcnt vmcnt(%0) lgkmcnt(0)\n\ts_barrier" ::"n"(n) : "memory")

DEVI u32x4 mksrd(const void* p, u32 size) {
  u64 a = (u64)p;
  return u32x4{(u32)a, (u32)(a >> 32) & 0xffffu, size, 0x00020000u};
}
typedef __amdgpu_buffer_rsrc_t rsrc_t;
DEVI rsrc_t mkrs(const void* p, u32 size) {
  return __builtin_amdgcn_make_buffer_rsrc((void*)p, 0, size, 0x00020000);
}
constexpr int SC1 = 16;
constexpr u32 SPIN_MAX = 1u << 18;

struct Args {
  const char* x; const char* wqkv; const char* wo; const char* wg; const char* wu; const char* wd;
  char* out; char* ws;
};

DEVI u64 ld_flag(const char* ws, u32 off) {
  return __hip_atomic_load((GLB_AS u64*)(ws + off), __ATOMIC_RELAXED, __HIP_MEMORY_SCOPE_AGENT);
}
DEVI void st_flag(char* ws, u32 off, u64 v) {
  __hip_atomic_store((GLB_AS u64*)(ws + off), v, __ATOMIC_RELAXED, __HIP_MEMORY_SCOPE_AGENT);
}
DEVI void poll_flags(const char* ws, u32 off, int lane, int n, u64 tag) {
  u32 o = off + (u32)(lane < n ? lane : 0) * 8u;
  for (u32 it = 0; it < SPIN_MAX; ++it) {
    u64 f = ld_flag(ws, o);
    if (__all(f == tag)) break;
  }
  asm volatile("" ::: "memory");
}

#define TS(i) do {} while (0)

constexpr int NB1 = 2;
constexpr int NB2 = 6;
constexpr int NBP = NB1 + NB2;

struct Frag { u32x4 x[5]; u32x4 xs; u32x2 wq[2], wk[2], wv[2]; };

DEVI void wgu_dma(int j, int w, int fb, u32 voff_g, u32x4 srd_g, u32x4 srd_u) {
  int idx, mat, grp;
  if (w == 3 || j < 16) {
    idx = w * 16 + j;
    mat = idx >= 40; grp = idx - mat * 40;
  } else {
    idx = w * 16 + (j - 16);
    mat = idx >= 24; grp = 40 + idx - mat * 24;
  }
  u32 lds = grp < 40 ? 49152u + mat * 40960u + grp * 1024u : mat * 24576u + (grp - 40) * 1024u;
  u32 soff = (u32)grp * (8u * 3584u) + (u32)fb * 128u;
  u32x4 srd = mat ? srd_u : srd_g;
  DMA16(lds, voff_g, srd, soff);
}

template <bool DO_P3>
DEVI void run_tail(const Args& A, const int lane, const int w, const int xcd, const int sl, const u64 tag,
                   const u32x4 (&B2)[4][4]
);

__global__ __launch_bounds__(256, 1) void fused_block_kernel(Args A) {
  __shared__ __attribute__((aligned(1024))) char smem[163840];
  asm volatile("" ::"v"((LDS_AS char*)smem) : "memory");
  const int tid = threadIdx.x, lane = tid & 63;
  const int w = __builtin_amdgcn_readfirstlane(tid >> 6);
  const int b = blockIdx.x, xcd = b & 7, sl = b >> 3;
  TS(0);
  const int t16 = lane & 15, q4 = lane >> 4, r8 = lane >> 3, c16 = lane & 7, qp = (lane & 15) >> 2;

  const int rg = 8 * (xcd >> 1) + (sl & 7), hq = 4 * (xcd & 1) + (sl >> 3);
  const u32x4 srd_x = mksrd(A.x, 2048u * 1024u);
  const u32x4 srd_qkv = mksrd(A.wqkv, 512u * 3072u);
  const u32 voff_x = (u32)q4 * 1024u + (u32)((t16 ^ ((4 * w + q4) & 15)) * 16);
  const u32 fW = ((r8 >> 1) & 1) | ((w & 1) << 1);
  const u32 voff_w = (u32)r8 * 3072u + (u32)((((c16 >> 1) ^ fW) * 2 + (c16 & 1)) * 16);
  const int row0 = 64 * rg - 16;
  auto dma_x = [&](int q) {
#pragma unroll
    for (int m = 0; m < 5; m++) {
      int i = w + 4 * m;
      int gr = row0 + 4 * i;
      if (m == 0 && gr < 0) gr += 16;
      DMA16((u32)(q * 20480 + i * 1024), voff_x, srd_x, (u32)(gr * 1024 + q * 256));
    }
  };
  const u32 wl0 = L_RING + (u32)w * 3072u;
  const u32 wg0 = (u32)(8 * w) * 3072u + (u32)hq * 128u;
  auto dma_w_all = [&](int st) {
#pragma unroll
    for (int m = 0; m < 2; m++)
      DMA16_TRIPLE(wl0 + (u32)((st % 3) * 24576 + m * 12288), voff_w, srd_qkv, wg0 + (u32)((64 * st + 32 * m) * 3072));
  };
  asm volatile("s_nop 4" ::: "memory");
  dma_x(0); dma_w_all(0); dma_w_all(1); dma_x(1); dma_w_all(2);
  const u32 sink = L_SCR + (u32)w * 2048u + 1024u;
  {
    u64 pc;
    asm volatile("s_getpc_b64 %0" : "=s"(pc));
    u32x4 srd_c = u32x4{(u32)pc & ~127u, (u32)(pc >> 32) & 0xffffu, 49152u, 0x00020000u};
    if (w < 2) {
      int li = (sl & 7) * 120 + w * 60 + (lane < 60 ? lane : 0);
      int row = 192 + li / 3, p = li % 3;
      u32 vo = (u32)(row * 3072 + p * 1024 + hq * 128);
      DMA4(sink, vo, srd_qkv, 0u);
    } else {
      u32 vo = (u32)lane * 128u + (u32)(w - 2) * 24576u;
      DMA4(sink, vo, srd_c, 0u);
      DMA4(sink, vo, srd_c, 8192u);
      DMA4(sink, vo, srd_c, 16384u);
    }
  }
  constexpr int M = 1;
  dma_x(2); dma_x(3);
  const u64 epoch = ld_flag(A.ws, WS_EPOCH);
  TS(40);

  u32 xb[4], xsb[4];
#pragma unroll
  for (int a = 0; a < 4; a++) {
    xb[a] = (u32)t16 * 256u + (u32)((((a << 2) | q4) ^ t16) << 4);
    xsb[a] = xb[a] + (u32)(w + 1) * 4096u;
  }
  const u32 fR = ((lane >> 3) & 1) | (((lane >> 4) & 1) << 1);
  const u32 wbase = L_RING + (u32)q4 * 3072u + (u32)qp * 128u + (u32)(((u32)w ^ fR) * 32u) + (u32)(lane & 3) * 8u;
  const u32 ONE2 = 0x3f803f80u;
  const u32x4 ones = {ONE2, ONE2, ONE2, ONE2};

  f32x4 aq[4], ak[5], av[5], aqc, ag, as, ag0;
  const f32x4 z4 = {0.f, 0.f, 0.f, 0.f};
#pragma unroll
  for (int i = 0; i < 4; i++) aq[i] = z4;
#pragma unroll
  for (int i = 0; i < 5; i++) { ak[i] = z4; av[i] = z4; }
  aqc = z4; ag = z4; as = z4; ag0 = z4;

  auto rd1 = [&](auto KS, auto I, Frag& F) {
    constexpr int ks = decltype(KS)::v, i = decltype(I)::v;
    constexpr u32 xo = (ks >> 2) * 20480;
    constexpr u32 wo = ((ks >> 1) % 3) * 24576 + (ks & 1) * 12288;
    if constexpr (i == 0) F.wq[0] = lds_tr(wbase + wo);
    if constexpr (i == 1) F.wq[1] = lds_tr(wbase + wo + 512);
    if constexpr (i >= 2 && i <= 5) F.x[i - 1] = lds_r128(xb[ks & 3] + xo + (i - 1) * 4096);
    if constexpr (i == 6) F.wk[0] = lds_tr(wbase + wo + 1024);
    if constexpr (i == 7) F.wk[1] = lds_tr(wbase + wo + 1024 + 512);
    if constexpr (i == 8) F.wv[0] = lds_tr(wbase + wo + 2048);
    if constexpr (i == 9) F.wv[1] = lds_tr(wbase + wo + 2048 + 512);
    if constexpr (i == 10) F.x[0] = lds_r128(xb[ks & 3] + xo);
    if constexpr (i == 11) F.xs = lds_r128(xsb[ks & 3] + xo);
  };
  auto mm1 = [&](auto I, Frag& F) {
    constexpr int i = decltype(I)::v;
    u32x4 wq = cat(F.wq[0], F.wq[1]), wk = cat(F.wk[0], F.wk[1]), wv = cat(F.wv[0], F.wv[1]);
    if constexpr (i < 4) aq[i] = mma16(wq, F.x[i + 1], aq[i]);
    else if constexpr (i < 8) ak[i - 3] = mma16(wk, F.x[i - 3], ak[i - 3]);
    else if constexpr (i < 12) av[i - 7] = mma16(F.x[i - 7], wv, av[i - 7]);
    else if constexpr (i == 12) aqc = mma16(wq, F.x[0], aqc);
    else if constexpr (i == 13) ak[0] = mma16(wk, F.x[0], ak[0]);
    else if constexpr (i == 14) av[0] = mma16(F.x[0], wv, av[0]);
    else if constexpr (i == 15) ag0 = mma16(F.x[0], F.x[0], ag0);
    else if constexpr (i == 16) ag = mma16(F.xs, F.xs, ag);
    else as = mma16(ones, F.xs, as);
  };

  const int rt = 4 * xcd + (sl & 3), ct = sl >> 2;
  const u32x4 srd_o = mksrd(A.wo, 512u * 1024u);
  const u32x4 srd_g = mksrd(A.wg, 512u * 3584u);
  const u32x4 srd_u = mksrd(A.wu, 512u * 3584u);
  const u32 voff_b = (u32)r8 * 1024u + (u32)((((c16 >> 1) ^ ((r8 >> 1) & 3)) * 2 + (c16 & 1)) * 16);
  const u32 b2part = w == 3 ? L_TOP : (u32)w * 16384u;
  const bool p3 = sl < 28;
  const int fb = p3 ? 7 * (xcd & 3) + (sl >> 2) : 0;

  Frag FA, FB;
  sfor<0, 8>([&](auto ST) {
    constexpr int st = decltype(ST)::v;
    constexpr int allow = st == 0 ? 6 + 5 + 6 + M + 11 : st == 1 ? 5 + 6 + M + 11 : st == 2 ? M + 11 + 6 : st == 3 ? 6
                        : st == 4 ? 6 : st == 5 ? 6 : st == 6 ? 8 : 18;
    WAIT_VM(allow);
    if constexpr ((st & 1) == 0) lds_w128((u32)((st >> 1) * 20480) + (u32)t16 * 16u, ones);
    BAR();
    if constexpr (st == 0) TS(1);
    if constexpr (st > 0) TS(16 + st);
    auto vm = [&](auto J) {
      constexpr int j = decltype(J)::v;
      if constexpr (st >= 1 && st <= 5 && j < 6) {
        constexpr int m = j / 3, p = j % 3, s2 = st + 2;
        DMA16o<p * 1024>(wl0 + (u32)((s2 % 3) * 24576 + m * 12288), voff_w, srd_qkv, wg0 + (u32)((64 * s2 + 32 * m) * 3072));
      }
      if constexpr (st == 5 && j == 6) {
        u32 vg = (u32)((sl & 3) * 128 + (w & 1) * 64 + lane) * 3584u + (u32)fb * 128u;
        if (w < 2) DMA4(sink, vg, srd_g, 0u); else DMA4(sink, vg, srd_u, 0u);
      }
      if constexpr (st == 5 && j == 7) {
        u32 vo = (u32)((sl & 3) * 128 + (w & 1) * 64 + lane) * 1024u + (u32)ct * 128u;
        DMA4(sink, vo, srd_o, 0u);
      }
      if constexpr (st == 6 && j < 16)
        DMA16(b2part + (u32)j * 1024u, voff_b, srd_o, (u32)((128 * w + 8 * j) * 1024 + ct * 128));
    };
    sfor<0, 18>([&](auto I) {
      constexpr int i = decltype(I)::v;
      if constexpr (i < 12) rd1(IC<2 * st>{}, I, FA);
      if constexpr (st > 0) mm1(I, FB);
      if constexpr (i >= 2 && (i & 1) == 0) vm(IC<(i - 2) / 2>{});
      SB();
    });
    sfor<0, 18>([&](auto I) {
      constexpr int i = decltype(I)::v;
      mm1(I, FA);
      if constexpr (i < 12) rd1(IC<2 * st + 1>{}, I, FB);
      if constexpr (i >= 2 && (i & 1) == 0) vm(IC<8 + (i - 2) / 2>{});
      SB();
    });
  });
  sfor<0, 18>([&](auto I) { mm1(IC<17 - decltype(I)::v>{}, FB); });
  TS(2);

  {
    auto fin = [&](float s, float ss, u32 addr, bool wr) {
      float mean = s * (1.f / 512.f);
      float var = fmaxf(ss * (1.f / 512.f) - mean * mean, 0.f);
      float rstd = __builtin_amdgcn_rsqf(var + 1e-5f);
      if (wr) lds_w64(addr, u32x2{asu(rstd), asu(-mean * rstd)});
    };
    const int i3 = lane & 3;
    const bool diag = q4 == qp;
    float ssd = i3 == 0 ? ag[0] : i3 == 1 ? ag[1] : i3 == 2 ? ag[2] : ag[3];
    fin(as[0], ssd, L_SCR + (u32)(16 * (w + 1) + t16) * 8u, diag);
    float ss0 = i3 == 0 ? ag0[0] : i3 == 1 ? ag0[1] : i3 == 2 ? ag0[2] : ag0[3];
    float s0 = asf((u32)__builtin_amdgcn_ds_bpermute(t16 * 4, (int)asu(ag0[0])));
    fin(s0, ss0, L_SCR + (u32)t16 * 8u, diag && w == 0);
  }
  BAR();
  TS(41);
  const u32 voff_g = (u32)r8 * 3584u + (u32)((((c16 >> 2) ^ ((r8 >> 1) & 1)) * 4 + (c16 & 3)) * 16);
  if (p3) {
#pragma unroll
    for (int j = 0; j < 8; ++j) wgu_dma(j, w, fb, voff_g, srd_g, srd_u);
  }

  f32x4 csq, csk;
#pragma unroll
  for (int i = 0; i < 4; i++) {
    csq[i] = asf((u32)__builtin_amdgcn_update_dpp(0, (int)asu(aqc[i]), 0x150, 0xf, 0xf, false));
    csk[i] = asf((u32)__builtin_amdgcn_update_dpp(0, (int)asu(ak[0][i]), 0x150, 0xf, 0xf, false));
  }
  float csv = asf((u32)__builtin_amdgcn_ds_bpermute(t16 * 4, (int)asu(av[0][0])));
  const float QS = 0.25f * 1.4426950408889634f;
  u32x2 qf[4], kf[5], vf[5];
#pragma unroll
  for (int T = 0; T < 5; T++) {
    u32x2 st = lds_r64(L_SCR + (u32)(16 * T + t16) * 8u);
    float rs = asf(st.x), nm = asf(st.y);
    f32x4 kk = ak[T] * rs + csk * nm;
    kf[T] = u32x2{pk2(kk[0], kk[1]), pk2(kk[2], kk[3])};
    if (T > 0) {
      f32x4 qq = aq[T - 1] * (rs * QS) + csq * (nm * QS);
      qf[T - 1] = u32x2{pk2(qq[0], qq[1]), pk2(qq[2], qq[3])};
    }
    u32x4 sa = lds_r128(L_SCR + (u32)(16 * T + 4 * q4) * 8u);
    u32x4 sb = lds_r128(L_SCR + (u32)(16 * T + 4 * q4) * 8u + 16u);
    float v0 = av[T][0] * asf(sa.x) + csv * asf(sa.y);
    float v1 = av[T][1] * asf(sa.z) + csv * asf(sa.w);
    float v2 = av[T][2] * asf(sb.x) + csv * asf(sb.y);
    float v3 = av[T][3] * asf(sb.z) + csv * asf(sb.w);
    vf[T] = u32x2{pk2(v0, v1), pk2(v2, v3)};
  }
  const float NEG = -1e30f;
  f32x4 bC, bP, bP1;
#pragma unroll
  for (int i = 0; i < 4; i++) {
    int kap = 4 * q4 + i;
    bool par = ((t16 - kap) & 1) == 0;
    bC[i] = (par && kap <= t16) ? 0.f : NEG;
    bP[i] = (par && kap >= t16 + 2) ? 0.f : NEG;
    bP1[i] = rg == 0 ? NEG : bP[i];
  }
  const u32 half = (t16 & 1) ? 0xffff0000u : 0x0000ffffu;
  u32 mC[2], mP[2];
#pragma unroll
  for (int a = 0; a < 2; a++) {
    bool isc = (4 * q4 + 2 * a + (t16 & 1)) <= t16;
    mC[a] = isc ? half : 0u; mP[a] = isc ? 0u : half;
  }
  float e0[4], e1[4], mx[4];
#pragma unroll
  for (int j = 0; j < 4; j++) {
    f32x4 sp = mma16k16(kf[j], qf[j], j == 0 ? bP1 : bP);
    f32x4 sc = mma16k16(kf[j + 1], qf[j], bC);
    e0[j] = fmaxf(fmaxf(sp[0], sp[1]), fmaxf(sc[0], sc[1]));
    e1[j] = fmaxf(fmaxf(sp[2], sp[3]), fmaxf(sc[2], sc[3]));
    mx[j] = fmaxf(e0[j], e1[j]);
  }
  {
    auto s01 = __builtin_amdgcn_permlane16_swap(asu(mx[0]), asu(mx[1]), false, false);
    auto s23 = __builtin_amdgcn_permlane16_swap(asu(mx[2]), asu(mx[3]), false, false);
    float ab = fmaxf(asf(s01[0]), asf(s01[1]));
    float cd = fmaxf(asf(s23[0]), asf(s23[1]));
    auto s2 = __builtin_amdgcn_permlane32_swap(asu(ab), asu(cd), false, false);
    float R = fmaxf(asf(s2[0]), asf(s2[1]));
    auto r2 = __builtin_amdgcn_permlane32_swap(asu(R), asu(R), false, false);
    auto ra = __builtin_amdgcn_permlane16_swap(r2[0], r2[0], false, false);
    auto rc = __builtin_amdgcn_permlane16_swap(r2[1], r2[1], false, false);
    mx[0] = asf(ra[0]); mx[1] = asf(ra[1]); mx[2] = asf(rc[0]); mx[3] = asf(rc[1]);
  }
  u32x2 ob[4];
#pragma unroll
  for (int j = 0; j < 4; j++) {
    float p0 = __builtin_amdgcn_exp2f(e0[j] - mx[j]);
    float p1 = __builtin_amdgcn_exp2f(e1[j] - mx[j]);
    u32 k0 = pk2(p0, p0), k1 = pk2(p1, p1);
    u32x4 P = {k0 & mP[0], k1 & mP[1], k0 & mC[0], k1 & mC[1]};
    f32x4 o = mma16(cat(vf[j], vf[j + 1]), P, z4);
    f32x4 sm = mma16(ones, P, z4);
    float inv = __builtin_amdgcn_rcpf(sm[0]);
    o = o * inv;
    ob[j] = u32x2{pk2(o[0], o[1]), pk2(o[2], o[3])};
  }
  const rsrc_t rs_ws = mkrs(A.ws, WS_SIZE);
  const int head = 4 * hq + w;
  const u32 oo = WS_OBUF + (u32)((2 * rg) * 32 + head) * 1024u;
  __builtin_amdgcn_raw_buffer_store_b128(cat(ob[0], ob[1]), rs_ws, lane * 16, oo, SC1);
  __builtin_amdgcn_raw_buffer_store_b128(cat(ob[2], ob[3]), rs_ws, lane * 16, oo + 32768u, SC1);
  TS(3);
  WAIT_VM(2);
  const u32 f2 = ((q4 & 1) << 1) | (qp >> 1);
  u32x4 B2[4][4];
#pragma unroll
  for (int c = 0; c < 4; c++) {
    u32 bc = b2part + (u32)(4 * q4 + qp) * 128u + (u32)(((u32)c ^ f2) * 32u) + (u32)(lane & 3) * 8u;
#pragma unroll
    for (int ks = 0; ks < 4; ks++) B2[ks][c] = cat(lds_tr(bc + ks * 4096), lds_tr(bc + ks * 4096 + 2048));
  }
  const u64 tag = epoch + 0x9E3779B97F4A7C15ull;
  if (p3) {
#pragma unroll
    for (int j = 8; j < 16; ++j) wgu_dma(j, w, fb, voff_g, srd_g, srd_u);
    WAIT_VM(8);
  } else {
    WAIT_VM(0);
  }
  if (lane == 0) st_flag(A.ws, WS_F1 + (u32)(rg * 32 + head) * 8u, tag);
  TS(4);
  BAR();
  if (p3) {
#pragma unroll
    for (int j = 16; j < 32; ++j) wgu_dma(j, w, fb, voff_g, srd_g, srd_u);
  }
  if (p3) run_tail<true>(A, lane, w, xcd, sl, tag, B2);
  else run_tail<false>(A, lane, w, xcd, sl, tag, B2);
}

template <bool DO_P3>
DEVI void run_tail(const Args& A, const int lane, const int w, const int xcd, const int sl, const u64 tag,
                   const u32x4 (&B2)[4][4]
) {
  const int t16 = lane & 15, q4 = lane >> 4, r8 = lane >> 3, c16 = lane & 7, qp = (lane & 15) >> 2;
  const int rt = 4 * xcd + (sl & 3), ct = sl >> 2;
  const rsrc_t rs_ws = mkrs(A.ws, WS_SIZE);
  const rsrc_t rs_x = mkrs(A.x, 2048u * 1024u);
  const rsrc_t rs_d = mkrs(A.wd, 1792u * 1024u);
  const rsrc_t rs_out = mkrs(A.out, 2048u * 1024u);
  const f32x4 z4 = {0.f, 0.f, 0.f, 0.f};
  const int utt = w >> 1, uG = w & 1;
  const u32 ONE2 = 0x3f803f80u;
  const u32x4 ones = {ONE2, ONE2, ONE2, ONE2};

  const u32 f2 = ((q4 & 1) << 1) | (qp >> 1);
  const u32 ring = L_TOP + (u32)w * 8192u;
  const u32 voff_d = (u32)r8 * 1024u + (u32)c16 * 16u;
  const u32 wr_d = ring + (u32)r8 * 128u + (u32)((((c16 >> 1) ^ ((r8 >> 1) & 3)) * 2 + (c16 & 1)) * 16);
  u32 b4c[4];
#pragma unroll
  for (int c = 0; c < 4; c++)
    b4c[c] = ring + (u32)(16 * (q4 >> 1) + 4 * (q4 & 1) + qp) * 128u + (u32)(((u32)c ^ f2) * 32u) + (u32)(lane & 3) * 8u;
  u32x2 B4[12][4][2];
  u32x4 tmp[14][4];
  auto b4_load1 = [&](auto J, auto I) {
    constexpr int j = decltype(J)::v, i = decltype(I)::v;
    tmp[j][i] = __builtin_amdgcn_raw_buffer_load_b128(rs_d, voff_d, (u32)((448 * w + 32 * j + 8 * i) * 1024 + ct * 128), 0);
  };
  auto b4_load = [&](auto J) { sfor<0, 4>([&](auto I) { b4_load1(J, I); }); };
  auto b4_op = [&](auto J, auto O) {
    constexpr int j = decltype(J)::v, o = decltype(O)::v;
    if constexpr (o < 4) lds_w128(wr_d + (j & 1) * 4096 + o * 1024, tmp[j][o]);
    else if constexpr (j < 12) B4[j][(o - 4) >> 1][o & 1] = lds_tr(b4c[(o - 4) >> 1] + (j & 1) * 4096 + (o & 1) * 1024);
  };

  u32x4 hres[2];
  {
    u32x2 xr[2][2];
#pragma unroll
    for (int s = 0; s < 2; s++)
#pragma unroll
      for (int c = 0; c < 2; c++)
        xr[s][c] = __builtin_amdgcn_raw_buffer_load_b64(rs_x, (u32)t16 * 1024u + (u32)q4 * 8u + (u32)c * 32u,
                                                       (u32)((64 * rt + 32 * s + 16 * utt) * 1024 + ct * 128 + uG * 64), 0);
    sfor<0, NB1>([&](auto J) { b4_load(J); });
    SB();
    poll_flags(A.ws, WS_F1 + (u32)(rt * 32 + 8 * w) * 8u, lane, 8, tag);
    TS(5);
    u32x4 ol[2][8];
#pragma unroll
    for (int s = 0; s < 2; s++)
#pragma unroll
      for (int i = 0; i < 8; i++)
        ol[s][i] = __builtin_amdgcn_raw_buffer_load_b128(rs_ws, lane * 16 + (i & 3) * 1024, WS_OBUF + (u32)((2 * rt + s) * 32 + 8 * w + (i & 4)) * 1024u, SC1);
    SB();
    const u32 ibw = L_TOP + (u32)w * 2048u + (u32)lane * 16u;
    const u32 ibr = L_TOP + (u32)w * 8192u + (u32)lane * 16u;
    f32x4 acc[2][4];
    auto gemm = [&](auto S) {
      constexpr int s = decltype(S)::v;
#pragma unroll
      for (int ks = 0; ks < 4; ks++) {
        u32x4 La = ol[s][2 * ks], Lb = ol[s][2 * ks + 1];
        u32x4 b0 = {La.x, La.y, Lb.x, Lb.y}, b1 = {La.z, La.w, Lb.z, Lb.w};
#pragma unroll
        for (int c = 0; c < 4; c++) {
          acc[0][c] = mma16(B2[ks][c], b0, ks == 0 ? z4 : acc[0][c]);
          acc[1][c] = mma16(B2[ks][c], b1, ks == 0 ? z4 : acc[1][c]);
        }
      }
    };
    auto put = [&]() {
#pragma unroll
      for (int u = 0; u < 4; u++)
#pragma unroll
        for (int c = 0; c < 2; c++)
          lds_w128(ibw + u * 8192 + c * 1024, __builtin_bit_cast(u32x4, acc[u >> 1][2 * (u & 1) + c]));
    };
    f32x4 pp[2][4];
    auto get = [&]() {
#pragma unroll
      for (int c = 0; c < 2; c++)
#pragma unroll
        for (int q = 0; q < 4; q++) pp[c][q] = __builtin_bit_cast(f32x4, lds_r128(ibr + q * 2048 + c * 1024));
    };
    auto fin = [&](auto S) {
      constexpr int s = decltype(S)::v;
      f32x4 r[2];
#pragma unroll
      for (int c = 0; c < 2; c++) {
        r[c] = f32x4{bflo(xr[s][c].x), bfhi(xr[s][c].x), bflo(xr[s][c].y), bfhi(xr[s][c].y)};
        r[c] += (pp[c][0] + pp[c][1]) + (pp[c][2] + pp[c][3]);
      }
      u32x4 h = {pk2(r[0][0], r[0][1]), pk2(r[0][2], r[0][3]), pk2(r[1][0], r[1][1]), pk2(r[1][2], r[1][3])};
      const int r32 = 2 * rt + s;
      __builtin_amdgcn_raw_buffer_store_b128(h, rs_ws, (u32)(q4 >> 1) * 1024u + (u32)((q4 & 1) * 32 + t16) * 16u,
                                             WS_HBUF + (u32)(r32 * 32 + 2 * (2 * ct + uG)) * 1024u + (u32)utt * 256u, SC1);
      float sm = 0.f, sq = 0.f;
#pragma unroll
      for (int i = 0; i < 4; i++) {
        float a = bflo(h[i]), c = bfhi(h[i]);
        sm += a + c; sq += a * a + c * c;
      }
      hres[s] = h;
      OPQ(hres[s]);
      auto x1 = __builtin_amdgcn_permlane16_swap(asu(sm), asu(sq), false, false);
      float y = asf(x1[0]) + asf(x1[1]);
      auto x2 = __builtin_amdgcn_permlane32_swap(asu(y), asu(y), false, false);
      float zz = asf(x2[0]) + asf(x2[1]);
      if (lane < 32)
        __builtin_amdgcn_raw_buffer_store_b32(asu(zz), rs_ws, (u32)t16 * 8u + (u32)q4 * 4u,
                                              WS_HSTAT + (u32)((r32 * 16 + 2 * ct + uG) * 32 + 16 * utt) * 8u, SC1);
    };
    gemm(IC<0>{});
    put();
    TS(42);
    BAR();
    get();
    SB();
    gemm(IC<1>{});
    SB();
    fin(IC<0>{});
    BAR();
    put();
    BAR();
    get();
    fin(IC<1>{});
    TS(43);
    WAIT_VM(2);
    if (lane == 0) st_flag(A.ws, WS_F2 + (u32)((2 * rt) * 32 + ct * 4 + w) * 8u, tag);
    sfor<NB1, NB1 + 2>([&](auto J) { b4_load(J); });
    WAIT_VM(9);
    if (lane == 0) st_flag(A.ws, WS_F2 + (u32)((2 * rt + 1) * 32 + ct * 4 + w) * 8u, tag);
  }
  TS(6);

  u32x4 ml[14][2];
  auto m_load1 = [&](auto S, auto J, auto TT) {
    constexpr int s = decltype(S)::v, j = decltype(J)::v, tt = decltype(TT)::v;
    ml[j][tt] = __builtin_amdgcn_raw_buffer_load_b128(rs_ws, lane * 16 + (j & 3) * 1024,
                                                     WS_MBUF + (u32)(((2 * rt + s) * 2 + tt) * 56 + 14 * w + (j & ~3)) * 1024u, SC1);
  };
  auto m_load = [&](auto S, auto J) { m_load1(S, J, IC<0>{}); m_load1(S, J, IC<1>{}); };

  if constexpr (DO_P3) {
    const int rg3 = 4 * (xcd >> 2) + (sl & 3), fb = 7 * (xcd & 3) + (sl >> 2);
    sfor<NB1 + 2, NBP>([&](auto J) { b4_load(J); });
    SB();
    BAR();
    TS(44);
    float cs[4];
    {
      const int fi = q4 & 1, kh = q4 >> 1;
      const u32 tb = (u32)(4 * kh + qp) * 128u + (u32)((qp >> 1) * 64) + (u32)fi * 32u + (u32)(lane & 3) * 8u;
      f32x16 ca[4];
#pragma unroll
      for (int m = 0; m < 4; m++)
#pragma unroll
        for (int i = 0; i < 16; i++) ca[m][i] = 0.f;
      sfor<0, 8>([&](auto BI) {
        constexpr int bi = decltype(BI)::v, mat = bi >> 2;
        const int G = 4 * w + (bi & 3);
        const u32 reg = G < 10 ? 49152u + mat * 40960u + (u32)G * 4096u : mat * 24576u + (u32)(G - 10) * 4096u;
        const u32 r0 = tb + reg, r1 = (tb ^ 64u) + reg, wa = (u32)lane * 16u + reg;
        u32x4 f[4];
#pragma unroll
        for (int e = 0; e < 2; e++) {
          f[2 * e] = cat(lds_tr(r0 + e * 1024), lds_tr(r0 + e * 1024 + 2048));
          f[2 * e + 1] = cat(lds_tr(r1 + e * 1024), lds_tr(r1 + e * 1024 + 2048));
        }
#pragma unroll
        for (int q = 0; q < 4; q++) {
          lds_w128(wa + q * 1024, f[q]);
          ca[2 * mat + (q & 1)] = mma32(ones, f[q], ca[2 * mat + (q & 1)]);
        }
      });
      if (lane < 32) lds_w128(ring + (u32)lane * 16u, u32x4{asu(ca[0][0]), asu(ca[1][0]), asu(ca[2][0]), asu(ca[3][0])});
      BAR();
      f32x4 c4 = z4;
#pragma unroll
      for (int m = 0; m < 4; m++) c4 += __builtin_bit_cast(f32x4, lds_r128(L_TOP + (u32)m * 8192u + (u32)(lane & 31) * 16u));
      cs[0] = c4[0]; cs[1] = c4[1]; cs[2] = c4[2]; cs[3] = c4[3];
      BAR();
    }
    TS(7);

    const int r32a = 8 * rg3 + 2 * w;
    poll_flags(A.ws, WS_F2 + (u32)(r32a * 32) * 8u, lane, 32, tag);
    TS(8);
    u32x4 hx[32];
    float ek1[2], ek2[2], ev[2];
    u32x4 mfr[2];
    u32x2 stl[8];
    auto st_load = [&](auto S) {
      constexpr int s = decltype(S)::v;
#pragma unroll
      for (int i = 0; i < 8; i++)
        stl[i] = __builtin_amdgcn_raw_buffer_load_b64(rs_ws, (u32)(lane & 31) * 8u + (u32)(lane >> 5) * 2048u + (u32)i * 256u,
                                                     WS_HSTAT + (u32)((r32a + s) * 16 * 32) * 8u, SC1);
    };
    auto st_fin = [&](auto S) {
      constexpr int s = decltype(S)::v;
      float sm = 0.f, sq = 0.f;
#pragma unroll
      for (int i = 0; i < 8; i++) { sm += asf(stl[i].x); sq += asf(stl[i].y); }
      auto a = __builtin_amdgcn_permlane32_swap(asu(sm), asu(sm), false, false);
      auto c = __builtin_amdgcn_permlane32_swap(asu(sq), asu(sq), false, false);
      sm = asf(a[0]) + asf(a[1]); sq = asf(c[0]) + asf(c[1]);
      float mean = sm * (1.f / 512.f);
      float var = fmaxf(sq * (1.f / 512.f) - mean * mean, 0.f) + 1e-5f;
      float rstd = __builtin_amdgcn_rsqf(var);
      const float C1 = -2.f * 0.7978845608028654f * 1.4426950408889634f;
      const float C2 = C1 * 0.044715f;
      ek1[s] = C1 * rstd; ek2[s] = C2 * rstd * rstd * rstd; ev[s] = var;
      float m = -mean;
      u32 hi = pk2(m, m); float r1 = m - bflo(hi);
      u32 mi = pk2(r1, r1); float r2 = r1 - bflo(mi);
      u32 lo = pk2(r2, r2);
      u32x4 f = {(hi & 0xffffu) | (mi & 0xffff0000u), (lo & 0xffffu) | (hi & 0xffff0000u), (mi & 0xffffu) | (hi & 0xffff0000u), 0u};
      if (lane >= 32) f = u32x4{0u, 0u, 0u, 0u};
      mfr[s] = f;
      OPQ(mfr[s]); OPQ(ek1[s]); OPQ(ek2[s]); OPQ(ev[s]);
    };
    auto cs_frag = [&](float c) {
      u32 hi = pk2(c, c); float r1 = c - bflo(hi);
      u32 mi = pk2(r1, r1); float r2 = r1 - bflo(mi);
      u32 lo = pk2(r2, r2);
      u32x4 f = u32x4{hi, (hi & 0xffffu) | (mi & 0xffff0000u), (mi & 0xffffu) | (lo & 0xffff0000u), 0u};
      OPQ(f);
      return f;
    };
    auto h_load = [&](auto S, auto K) {
      constexpr int s = decltype(S)::v, k = decltype(K)::v;
      hx[k] = __builtin_amdgcn_raw_buffer_load_b128(rs_ws, lane * 16 + (k & 3) * 1024, WS_HBUF + (u32)((r32a + s) * 32 + (k & ~3)) * 1024u, SC1);
    };
    sfor<0, 8>([&](auto K) { h_load(IC<0>{}, K); });
    st_load(IC<0>{});
    sfor<8, 32>([&](auto K) { h_load(IC<0>{}, K); });
    SB();
    u32x4 csf[2];

    f32x16 ag[2], au[2];
    u32 mo[8];
    float ea[2], eb[2];
    auto epi = [&](auto P, auto E, auto Q) {
      constexpr int p = decltype(P)::v, e = decltype(E)::v, q = decltype(Q)::v;
      constexpr int a = p & 1, s = p >> 1;
      const float g0 = ag[a][2 * e], g1 = ag[a][2 * e + 1];
      if constexpr (q == 0) { ea[0] = g0 * g0; ea[1] = g1 * g1; ea[0] = __builtin_fmaf(ea[0], ek2[s], ek1[s]); }
      if constexpr (q == 1) { ea[1] = __builtin_fmaf(ea[1], ek2[s], ek1[s]); ea[0] *= g0; ea[1] *= g1; }
      if constexpr (q == 2) { ea[0] = __builtin_amdgcn_exp2f(ea[0]); eb[0] = g0 * au[a][2 * e]; }
      if constexpr (q == 3) { ea[1] = __builtin_amdgcn_exp2f(ea[1]); eb[1] = g1 * au[a][2 * e + 1]; }
      if constexpr (q == 4) { ea[0] = __builtin_amdgcn_rcpf(__builtin_fmaf(ea[0], ev[s], ev[s])); }
      if constexpr (q == 5) { ea[1] = __builtin_amdgcn_rcpf(__builtin_fmaf(ea[1], ev[s], ev[s])); }
      if constexpr (q == 6) { mo[e] = pk2(eb[0] * ea[0], eb[1] * ea[1]); }
      OPQ(ea[0]); OPQ(ea[1]); OPQ(eb[0]); OPQ(eb[1]);
    };
    const u32 mvo = (u32)((lane >> 4) & 1) * (56u * 1024u) + (u32)((lane >> 5) * 16 + t16) * 16u;
    auto epi_store = [&](auto P, auto AH) {
      constexpr int p = decltype(P)::v, ah = decltype(AH)::v;
      constexpr int fh = p & 1, s = p >> 1;
      u32x4 v = {mo[4 * ah], mo[4 * ah + 1], mo[4 * ah + 2], mo[4 * ah + 3]};
      __builtin_amdgcn_raw_buffer_store_b128(v, rs_ws, mvo + (u32)(fh * 1024 + ah * 512),
                                             WS_MBUF + (u32)((r32a + s) * 2 * 56 + 2 * fb) * 1024u, SC1);
    };
    auto f3_flag = [&](auto S) {
      constexpr int s = decltype(S)::v;
      if (lane == 0) st_flag(A.ws, WS_F3 + (u32)((r32a + s) * 32 + fb) * 8u, tag);
    };
    ea[0] = ea[1] = eb[0] = eb[1] = 0.f;

    u32x2 wbc;
    auto wfrag = [&](auto GI) {
      constexpr int G = decltype(GI)::v, fh = (G >> 6) & 1, k16 = (G >> 1) & 31, mat = G & 1;
      constexpr int Gp = k16 >> 1;
      constexpr u32 reg = (Gp < 10 ? 49152u + mat * 40960u + Gp * 4096u : mat * 24576u + (Gp - 10) * 4096u) + (2 * (k16 & 1) + fh) * 1024u;
      if constexpr ((G & 63) == 0) { wbc = u32x2{(u32)lane * 16u, (u32)lane * 16u + 65536u}; OPQ(wbc); }
      if constexpr (reg < 65536u) return lds_r128(wbc.x + reg);
      else return lds_r128(wbc.y + (reg - 65536u));
    };
    constexpr int WD = 4;
    u32x4 wf[WD + 1];
    sfor<0, WD>([&](auto I) { wf[decltype(I)::v] = wfrag(I); });
    u64 fl1 = 0;
    sfor<0, 256>([&](auto GI) {
      constexpr int G = decltype(GI)::v;
      constexpr int p = G >> 6, g = G & 63, k = g >> 1, mat = g & 1;
      constexpr int a = p & 1, fh = p & 1, s = p >> 1;
      if constexpr (G + WD < 256) wf[(G + WD) % (WD + 1)] = wfrag(IC<G + WD>{});
      {
        f32x16& acc = mat == 0 ? ag[a] : au[a];
        if constexpr (k == 0) MMA32_INIT(acc, wf[G % (WD + 1)], hx[k]);
        else if constexpr (k & 1) MMA32_ACC_A(acc, wf[G % (WD + 1)], hx[k]);
        else MMA32_ACC(acc, wf[G % (WD + 1)], hx[k]);
      }
      if constexpr (g == 62) MMA32_ACC(ag[a], csf[0], mfr[s]);
      if constexpr (g == 63) MMA32_ACC(au[a], csf[1], mfr[s]);
      if constexpr (g == 2) csf[0] = cs_frag(cs[fh]);
      if constexpr (g == 4) csf[1] = cs_frag(cs[2 + fh]);
      if constexpr (p > 0) {
        constexpr int E0 = 6;
        if constexpr (g >= E0 && g < E0 + 56) epi(IC<p - 1>{}, IC<(g - E0) / 7>{}, IC<(g - E0) % 7>{});
        if constexpr (g == E0 + 28) epi_store(IC<p - 1>{}, IC<0>{});
        if constexpr (g == E0 + 56) epi_store(IC<p - 1>{}, IC<1>{});
      }
      if constexpr (p == 0) {
        if constexpr (2 * g + 1 < 12 * NBP) {
          b4_op(IC<(2 * g) / 12>{}, IC<(2 * g) % 12>{});
          b4_op(IC<(2 * g + 1) / 12>{}, IC<(2 * g + 1) % 12>{});
        }
        if constexpr (g == 50) st_fin(IC<0>{});
        if constexpr (g == 44) fl1 = ld_flag(A.ws, WS_F2 + (u32)((r32a + 1) * 32 + (lane & 31)) * 8u);
      }
      if constexpr (p == 1) {
        if constexpr (g == 0) {
          if (!__all(fl1 == tag)) poll_flags(A.ws, WS_F2 + (u32)((r32a + 1) * 32) * 8u, lane, 32, tag);
          asm volatile("" ::: "memory");
          st_load(IC<1>{});
        }
        if constexpr (mat == 1) h_load(IC<1>{}, IC<k>{});
      }
      if constexpr (p == 2) {
        if constexpr (g == 0) st_fin(IC<1>{});
        constexpr int li = g >> 1;
        if constexpr (mat == 1 && NBP * 4 + li < 40) b4_load1(IC<NBP + li / 4>{}, IC<li % 4>{});
        constexpr int oi = g - 24;
        if constexpr (oi >= 0 && NBP * 12 + oi < 120) b4_op(IC<NBP + oi / 12>{}, IC<oi % 12>{});
        static_assert(NBP >= 7 && NBP <= 10, "");
      }
      if constexpr (p == 3) {
        constexpr int li = (g - 1) >> 1;
        if constexpr (mat == 1 && li < 16) b4_load1(IC<10 + li / 4>{}, IC<li % 4>{});
        constexpr int oi = g - 22;
        if constexpr (oi >= 0 && oi < 24) b4_op(IC<10 + oi / 12>{}, IC<oi % 12>{});
        if constexpr (oi >= 24 && oi < 32) b4_op(IC<12 + (oi - 24) / 4>{}, IC<(oi - 24) % 4>{});
        if constexpr (g == 14) { WAIT_VM(7); f3_flag(IC<0>{}); }
        if constexpr (g == 36) fl1 = ld_flag(A.ws, WS_F3 + (u32)((2 * rt) * 32 + 7 * w + (lane < 7 ? lane : 0)) * 8u);
        if constexpr (g == 50) {
          if (!__all(fl1 == tag)) poll_flags(A.ws, WS_F3 + (u32)((2 * rt) * 32 + 7 * w) * 8u, lane, 7, tag);
          asm volatile("" ::: "memory");
        }
        if constexpr (g >= 50) m_load(IC<0>{}, IC<g - 50>{});
      }
      if constexpr ((g & 15) == 15) TS(24 + 4 * p + (g >> 4));
      SB();
    });
    TS(10);
    asm volatile("s_nop 11" ::: "memory");
    SB();
    sfor<0, 8>([&](auto E) {
      constexpr int e = decltype(E)::v;
      float y[2];
#pragma unroll
      for (int i = 0; i < 2; i++) {
        float g = ag[1][2 * e + i], u = au[1][2 * e + i];
        float ex = __builtin_amdgcn_exp2f(g * __builtin_fmaf(g * g, ek2[1], ek1[1]));
        y[i] = (g * u) * __builtin_amdgcn_rcpf(__builtin_fmaf(ex, ev[1], ev[1]));
      }
      mo[e] = pk2(y[0], y[1]);
      if constexpr (e == 3) epi_store(IC<3>{}, IC<0>{});
      if constexpr (e == 7) epi_store(IC<3>{}, IC<1>{});
    });
    TS(45);
    WAIT_VM(0);
    f3_flag(IC<1>{});
    TS(11);
  } else {
    BAR();
    sfor<NB1 + 2, 14>([&](auto J) { b4_load(J); });
    SB();
    sfor<0, 14>([&](auto J) { sfor<0, 12>([&](auto O) { b4_op(J, O); }); });
    if (sl == 31 && xcd == 7) {
#pragma unroll 1
      for (int i = 0; i < 4; i++) {
        u32 o = WS_F1 + (u32)((w * 4 + i) * 64 + lane) * 8u;
        for (u32 it = 0; it < SPIN_MAX; ++it) {
          u64 f = ld_flag(A.ws, o);
          if (__all(f == tag)) break;
        }
      }
      BAR();
      if (w == 0 && lane == 0) st_flag(A.ws, WS_EPOCH, tag);
    }
    poll_flags(A.ws, WS_F3 + (u32)((2 * rt) * 32 + 7 * w) * 8u, lane, 7, tag);
    sfor<0, 14>([&](auto J) { m_load(IC<0>{}, J); });
    SB();
  }

  sfor<0, 2>([&](auto S) {
    constexpr int s = decltype(S)::v;
    if constexpr (s == 1) {
      poll_flags(A.ws, WS_F3 + (u32)((2 * rt + 1) * 32 + 7 * w) * 8u, lane, 7, tag);
      TS(13);
      sfor<0, 14>([&](auto J) { m_load(IC<1>{}, J); });
      SB();
    }
    f32x4 acc[2][4];
    u32x4 bl[2][4];
#pragma unroll
    for (int j = 0; j < 2; j++)
#pragma unroll
      for (int c = 0; c < 4; c++) bl[j][c] = cat(lds_tr(b4c[c] + j * 4096), lds_tr(b4c[c] + j * 4096 + 1024));
    sfor<0, 14>([&](auto J) {
      constexpr int j = decltype(J)::v;
#pragma unroll
      for (int c = 0; c < 4; c++) {
        u32x4 bw;
        if constexpr (j < 12) bw = cat(B4[j][c][0], B4[j][c][1]); else bw = bl[j - 12][c];
        acc[0][c] = mma16(bw, ml[j][0], j == 0 ? z4 : acc[0][c]);
        acc[1][c] = mma16(bw, ml[j][1], j == 0 ? z4 : acc[1][c]);
      }
    });
    const u32 ibw = (u32)s * 32768u + (u32)w * 2048u + (u32)lane * 16u;
    const u32 ibr = (u32)s * 32768u + (u32)w * 8192u + (u32)lane * 16u;
    if constexpr (s == 0) BAR();
#pragma unroll
    for (int u = 0; u < 4; u++)
#pragma unroll
      for (int c = 0; c < 2; c++)
        lds_w128(ibw + u * 8192 + c * 1024, __builtin_bit_cast(u32x4, acc[u >> 1][2 * (u & 1) + c]));
    BAR();
    u32x2 o2[2];
#pragma unroll
    for (int c = 0; c < 2; c++) {
      u32 ha = hres[s][2 * c], hb = hres[s][2 * c + 1];
      f32x4 r = f32x4{bflo(ha), bfhi(ha), bflo(hb), bfhi(hb)};
      f32x4 p0 = __builtin_bit_cast(f32x4, lds_r128(ibr + 0 * 2048 + c * 1024));
      f32x4 p1 = __builtin_bit_cast(f32x4, lds_r128(ibr + 1 * 2048 + c * 1024));
      f32x4 p2 = __builtin_bit_cast(f32x4, lds_r128(ibr + 2 * 2048 + c * 1024));
      f32x4 p3 = __builtin_bit_cast(f32x4, lds_r128(ibr + 3 * 2048 + c * 1024));
      r += (p0 + p1) + (p2 + p3);
      o2[c] = u32x2{pk2(r[0], r[1]), pk2(r[2], r[3])};
    }
    auto sx = __builtin_amdgcn_permlane16_swap(o2[0].x, o2[1].x, false, false);
    auto sy = __builtin_amdgcn_permlane16_swap(o2[0].y, o2[1].y, false, false);
    u32x4 ov = {sx[0], sy[0], sx[1], sy[1]};
    __builtin_amdgcn_raw_buffer_store_b128(ov, rs_out, (u32)t16 * 1024u + (u32)((q4 & 1) * 32 + (q4 >> 1) * 16),
                                           (u32)((64 * rt + 32 * s + 16 * utt) * 1024 + ct * 128 + uG * 64), 0);
    if constexpr (s == 0) TS(12);
  });
  TS(14);
}

extern "C" void kernel_launch(void* const* d_in, const int* in_sizes, int n_in, void* d_out, int out_size,
                              void* d_ws, size_t ws_size, hipStream_t stream) {
  Args a;
  a.x = (const char*)d_in[0]; a.wqkv = (const char*)d_in[1]; a.wo = (const char*)d_in[2];
  a.wg = (const char*)d_in[4]; a.wu = (const char*)d_in[5]; a.wd = (const char*)d_in[6];
  a.out = (char*)d_out; a.ws = (char*)d_ws;
  fused_block_kernel<<<256, 256, 0, stream>>>(a);
}
```

```cpp
#include <hip/hip_runtime.h>
#include <stdint.h>

typedef unsigned u32;
typedef unsigned long long u64;
typedef u32 u32x2 __attribute__((ext_vector_type(2)));
typedef u32 u32x4 __attribute__((ext_vector_type(4)));
typedef float f32x2 __attribute__((ext_vector_type(2)));
typedef float f32x4 __attribute__((ext_vector_type(4)));
typedef float f32x16 __attribute__((ext_vector_type(16)));
typedef short s16x4 __attribute__((ext_vector_type(4)));
typedef __bf16 bf16x2 __attribute__((ext_vector_type(2)));
typedef __bf16 bf16x8 __attribute__((ext_vector_type(8)));
#define LDS_AS __attribute__((address_space(3)))
#define GLB_AS __attribute__((address_space(1)))
#define DEVI __device__ __forceinline__

#define DBG_TS 0

template <int N> struct IC { static constexpr int v = N; };
template <int B, int E, class F> DEVI void sfor(F&& f) {
  if constexpr (B < E) { f(IC<B>{}); sfor<B + 1, E>(f); }
}

constexpr u32 WS_EPOCH = 0;
constexpr u32 WS_F1 = 4096;
constexpr u32 WS_F2 = 16384;
constexpr u32 WS_F3 = 32768;
constexpr u32 WS_HSTAT = 65536;
constexpr u32 WS_DBG = 524288;
constexpr u32 WS_OBUF = 1u << 20;
constexpr u32 WS_HBUF = 3u << 20;
constexpr u32 WS_MBUF = 5u << 20;
constexpr u32 WS_SIZE = 12u << 20;

constexpr u32 L_RING = 81920;
constexpr u32 L_SCR = 155648;
constexpr u32 L_TOP = 131072;

DEVI u32x4 lds_r128(u32 a) { return *(LDS_AS u32x4*)(size_t)(a); }
DEVI u32x2 lds_r64(u32 a) { return *(LDS_AS u32x2*)(size_t)(a); }
DEVI u32 lds_r32(u32 a) { return *(LDS_AS u32*)(size_t)(a); }
DEVI void lds_w128(u32 a, u32x4 v) { *(LDS_AS u32x4*)(size_t)(a) = v; }
DEVI void lds_w64(u32 a, u32x2 v) { *(LDS_AS u32x2*)(size_t)(a) = v; }
DEVI void lds_w32(u32 a, u32 v) { *(LDS_AS u32*)(size_t)(a) = v; }
DEVI u32x2 lds_tr(u32 a) {
  return __builtin_bit_cast(u32x2, __builtin_amdgcn_ds_read_tr16_b64_v4i16((LDS_AS s16x4*)(size_t)(a)));
}
DEVI u32x4 cat(u32x2 a, u32x2 b) { return u32x4{a.x, a.y, b.x, b.y}; }
DEVI f32x4 mma16(u32x4 a, u32x4 b, f32x4 c) {
  return __builtin_amdgcn_mfma_f32_16x16x32_bf16(__builtin_bit_cast(bf16x8, a), __builtin_bit_cast(bf16x8, b), c, 0, 0, 0);
}
DEVI f32x4 mma16k16(u32x2 a, u32x2 b, f32x4 c) {
  return __builtin_amdgcn_mfma_f32_16x16x16bf16_1k(__builtin_bit_cast(s16x4, a), __builtin_bit_cast(s16x4, b), c, 0, 0, 0);
}
DEVI f32x16 mma32(u32x4 a, u32x4 b, f32x16 c) {
  return __builtin_amdgcn_mfma_f32_32x32x16_bf16(__builtin_bit_cast(bf16x8, a), __builtin_bit_cast(bf16x8, b), c, 0, 0, 0);
}
DEVI u32 pk2(float a, float b) { bf16x2 v = {(__bf16)a, (__bf16)b}; return __builtin_bit_cast(u32, v); }
DEVI float bflo(u32 v) { return __uint_as_float(v << 16); }
DEVI float bfhi(u32 v) { return __uint_as_float(v & 0xffff0000u); }
DEVI float asf(u32 v) { return __uint_as_float(v); }
DEVI u32 asu(float v) { return __float_as_uint(v); }
#define SB() __builtin_amdgcn_sched_barrier(0)

DEVI void SETM0(u32 v) { asm volatile("s_mov_b32 m0, %0" ::"s"(v) : "memory"); }
DEVI void SETM0N(u32 v) { asm volatile("s_mov_b32 m0, %0\n\ts_nop 0" ::"s"(v) : "memory"); }
template <int OFF> DEVI void DMAn(u32 voff, u32x4 srd, u32 soff) {
  asm volatile("buffer_load_dwordx4 %0, %1, %2 offen offset:%3 lds" ::"v"(voff), "s"(srd), "s"(soff), "n"(OFF) : "memory");
}
DEVI void TOUCH(u32& dummy, u32 voff, u32x4 srd, u32 soff) {
  asm volatile("buffer_load_dword %0, %1, %2, %3 offen" : "+v"(dummy) : "v"(voff), "s"(srd), "s"(soff) : "memory");
}
DEVI void MMA32_INIT(f32x16& acc, const u32x4& a, const u32x4& b) {
  asm volatile("v_mfma_f32_32x32x16_bf16 %0, %1, %2, 0" : "=&v"(acc) : "v"(a), "v"(b));
}
DEVI void MMA32_ACC(f32x16& acc, const u32x4& a, const u32x4& b) {
  asm volatile("v_mfma_f32_32x32x16_bf16 %0, %1, %2, %0" : "+v"(acc) : "v"(a), "v"(b));
}
DEVI void MMA32_ACC_A(f32x16& acc, const u32x4& a, const u32x4& b) {
  asm volatile("v_mfma_f32_32x32x16_bf16 %0, %1, %2, %0" : "+v"(acc) : "v"(a), "a"(b));
}
DEVI void MMA16_INIT(f32x4& acc, const u32x4& a, const u32x4& b) {
  asm volatile("v_mfma_f32_16x16x32_bf16 %0, %1, %2, 0" : "=&v"(acc) : "v"(a), "v"(b));
}
DEVI void MMA16_ACC(f32x4& acc, const u32x4& a, const u32x4& b) {
  asm volatile("v_mfma_f32_16x16x32_bf16 %0, %1, %2, %0" : "+v"(acc) : "v"(a), "v"(b));
}
template <bool INIT> DEVI void MMA16(f32x4& acc, const u32x4& a, const u32x4& b) {
  if constexpr (INIT) MMA16_INIT(acc, a, b); else MMA16_ACC(acc, a, b);
}
DEVI float bcast_row0(float v) {
  auto a = __builtin_amdgcn_permlane32_swap(__float_as_uint(v), __float_as_uint(v), false, false);
  auto c = __builtin_amdgcn_permlane16_swap(a[0], a[0], false, false);
  return __uint_as_float(c[0]);
}
template <class T> DEVI void OPQ(T& v) { asm volatile("" : "+v"(v)); }
#define WAIT_VM(n) asm volatile("s_waitcnt vmcnt(%0)" ::"n"(n) : "memory")
#define WAIT_LGKM0() asm volatile("s_waitcnt lgkmcnt(0)" ::: "memory")
#define BAR() asm volatile("s_waitcnt lgkmcnt(0)\n\ts_barrier" ::: "memory")
#define BAR_VM(n) asm volatile("s_waitcnt vmcnt(%0) lgkmcnt(0)\n\ts_barrier" ::"n"(n) : "memory")

DEVI u32x4 mksrd(const void* p, u32 size) {
  u64 a = (u64)p;
  return u32x4{(u32)a, (u32)(a >> 32) & 0xffffu, size, 0x00020000u};
}
typedef __amdgpu_buffer_rsrc_t rsrc_t;
DEVI rsrc_t mkrs(const void* p, u32 size) {
  return __builtin_amdgcn_make_buffer_rsrc((void*)p, 0, size, 0x00020000);
}
constexpr int SC1 = 16;
constexpr u32 SPIN_MAX = 1u << 18;

struct Args {
  const char* x; const char* wqkv; const char* wo; const char* wg; const char* wu; const char* wd;
  char* out; char* ws;
};

DEVI u64 ld_flag(const char* ws, u32 off) {
  return __hip_atomic_load((GLB_AS u64*)(ws + off), __ATOMIC_RELAXED, __HIP_MEMORY_SCOPE_AGENT);
}
DEVI void st_flag(char* ws, u32 off, u64 v) {
  __hip_atomic_store((GLB_AS u64*)(ws + off), v, __ATOMIC_RELAXED, __HIP_MEMORY_SCOPE_AGENT);
}
DEVI void poll_flags(const char* ws, u32 off, int lane, int n, u64 tag) {
  u32 o = off + (u32)(lane < n ? lane : 0) * 8u;
  for (u32 it = 0; it < SPIN_MAX; ++it) {
    u64 f = ld_flag(ws, o);
    if (__all(f == tag)) break;
  }
  asm volatile("" ::: "memory");
}

#define TS(i) do {} while (0)

#define ST_TA 4
#define ST_TB 4
#define ST_TO 4
#define NBW1 8
#define NBW2 0
constexpr int NBP = NBW1 + NBW2;
constexpr u32 CODE_BYTES = 45056;

constexpr int b4_spos(int i) { int nf = NBP < 12 ? 12 - NBP : 0; return 128 + 8 + 12 * (i < nf ? i : nf) + 4 * (i > nf ? i - nf : 0); }
constexpr int b4_lpos(int i, int l) { return b4_spos(i) - 20 + (NBP + i < 12 ? 3 * l : l); }

enum { K_NONE = 0, K_M0X, K_X, K_M0W, K_W, K_T, K_M0G, K_G, K_E };
struct MList {
  int n = 0; int k[48] = {}, a[48] = {}, b[48] = {};
  constexpr void add(int kk, int aa, int bb) { k[n] = kk; a[n] = aa; b[n] = bb; n++; }
  constexpr void X(int q) { add(K_M0X, q, 0); for (int m = 0; m < 4; m++) add(K_X, q, m); add(K_M0X, q, 4); add(K_X, q, 4); }
  constexpr void W(int st) { for (int m = 0; m < 2; m++) { add(K_M0W, st, m); for (int p = 0; p < 3; p++) add(K_W, st, m * 3 + p); } }
  constexpr void T(int s) { add(K_T, s, 0); }
  constexpr void G(int s) { add(K_M0G, s, 0); for (int i = 0; i < 4; i++) add(K_G, s, i); }
};
#define GST {6, 6, 7, 8, 8, 8, 8, 8}
constexpr int gstage(int s) { constexpr int t[8] = GST; return t[s]; }
constexpr MList stage_ops(int st) {
  MList L;
  if (st == -2) { L.X(0); L.W(0); L.add(K_E, 0, 0); }
  if (st == -1) { L.W(1); }
  if (st == 0) { L.X(1); L.W(2); L.W(3); }
  if (st == 1) { L.W(4); L.X(2); }
  if (st == 2) { L.W(5); }
  if (st == 3) { L.W(6); }
  if (st == 4) { L.X(3); L.W(7); }
  if (st == ST_TO) L.T(7);
  if (st == ST_TA) L.T(5);
  if (st == ST_TB) L.T(6);
  for (int s = 0; s < 8; s++) if (gstage(s) == st) L.G(s);
  return L;
}
constexpr bool is_vm(int k) { return k == K_X || k == K_W || k == K_T || k == K_G || k == K_E; }
constexpr int vm_allow(int st) {
  int cnt = 0;
  for (int s = -2; s < st; s++) {
    MList L = stage_ops(s);
    for (int i = 0; i < L.n; i++) {
      if (!is_vm(L.k[i])) continue;
      cnt++;
      if ((L.k[i] == K_W && L.a[i] == st) || (L.k[i] == K_X && L.a[i] == (st >> 1))) cnt = 0;
    }
  }
  return cnt;
}
constexpr u32 wreg(int st) { return st == 7 ? 0u : st == 3 ? 61440u : L_RING + (u32)((st > 3 ? st - 4 : st) * 24576); }
constexpr int gap_cap(int st, int g, int n) {
  int i = g % 18;
  if (st == 0 && g < 18) return i >= 4 ? 1 : 0;
  if (i >= 12) return 1;
  if (n > 12 && (i == 5 || i == 6 || i == 7 || i == 9 || i == 10 || i == 11)) return 1;
  return 0;
}
constexpr int gap_first(int st, int g, int n) { int c = 0; for (int i = 0; i < g; i++) c += gap_cap(st, i, n); return c; }

DEVI u32x4 selsrd(bool c, u32x4 a, u32x4 b) { return u32x4{c ? a.x : b.x, c ? a.y : b.y, c ? a.z : b.z, a.w}; }
struct Frag { u32x4 x[5]; u32x4 xs; u32x2 wq[2], wk[2], wv[2]; };

template <bool DO_P3>
DEVI void run_tail(const Args& A, const int lane, const int w, const int xcd, const int sl, const u64 tag,
                   u32x4 (&tb2)[4][4]
);

DEVI int gblock(int s, int w) {
  return s == 0 ? 6 + w : s == 1 ? 20 + w : s == 2 ? 10 + w : s == 3 ? 24 + w : s == 4 ? 28 + w : s == 5 ? 16 + w
       : s == 6 ? w : (w < 2 ? 4 + w : 12 + w);
}
constexpr int gmat(int s) { return (s == 1 || s == 3 || s == 4 || s == 5) ? 1 : 0; }

__global__ __launch_bounds__(256, 1) void fused_block_kernel(Args A) {
  __shared__ __attribute__((aligned(1024))) char smem[163840];
  asm volatile("" ::"v"((LDS_AS char*)smem) : "memory");
  const int tid = threadIdx.x, lane = tid & 63;
  const int w = __builtin_amdgcn_readfirstlane(tid >> 6);
  const int b = blockIdx.x, xcd = b & 7, sl = b >> 3;
  TS(0);
  const int t16 = lane & 15, q4 = lane >> 4, r8 = lane >> 3, c16 = lane & 7, qp = (lane & 15) >> 2;
  u32 tv = 0;
  {
    u64 pc;
    asm volatile("s_getpc_b64 %0" : "=s"(pc));
    u32x4 srd_c = u32x4{(u32)pc & ~127u, (u32)(pc >> 32) & 0xffffu, CODE_BYTES, 0x00020000u};
    TOUCH(tv, (u32)(((lane & 3) * 128 + sl * 4 + w) * 128), srd_c, 0u);
  }
  SB();
  TS(38);

  const int rg = 8 * (xcd >> 1) + (sl & 7), hq = 4 * (xcd & 1) + (sl >> 3);
  const int rt = 4 * xcd + (sl & 3), ct = sl >> 2;
  const bool p3 = sl < 28;
  const int fb = p3 ? 7 * (xcd & 3) + (sl >> 2) : 7 * (xcd & 3);
  const u32x4 srd_x = mksrd(A.x - 4096, 2048u * 1024u + 4096u);
  const u32x4 srd_qkv = mksrd(A.wqkv, 512u * 3072u);
  const u32x4 srd_o = mksrd(A.wo, 512u * 1024u);
  const u32x4 srd_g = mksrd(A.wg, 512u * 3584u);
  const u32x4 srd_u = mksrd(A.wu, 512u * 3584u);
  const int row0 = 64 * rg - 16;
  u32 voff_x[5];
#pragma unroll
  for (int m = 0; m < 5; m++) {
    int gr = row0 + 16 * m + 4 * w;
    if (m == 0 && gr < 0) gr += 16;
    voff_x[m] = (u32)((gr + q4 + 4 - (m & 3) * (m < 4 ? 1 : 0)) * 1024) + (u32)((t16 ^ ((4 * w + q4) & 15)) * 16);
  }
  const u32 fW = ((r8 >> 1) & 1) | ((w & 1) << 1);
  const u32 voff_w = (u32)(8 * w + r8) * 3072u + (u32)hq * 128u + (u32)((((c16 >> 1) ^ fW) * 2 + (c16 & 1)) * 16);
  const u32 voff_w1 = voff_w + 32u * 3072u;
  u32 voff_g[4];
  const u32 rq = (u32)(((sl & 7) >> 1) + (sl >> 3)), rp = (u32)(sl & 1);
  u32 sW[8], sX[4];
  auto rot = [&](int i0, int i1) {
#pragma unroll
    for (int q = i0; q < i1; q++) {
      u32 pq = (q + rq) & 3u;
      sX[q] = pq * 256u;
      sW[2 * q] = (2u * pq + rp) * (64u * 3072u);
      sW[2 * q + 1] = (2u * pq + (rp ^ 1u)) * (64u * 3072u);
      asm volatile("" : "+s"(sX[q]), "+s"(sW[2 * q]), "+s"(sW[2 * q + 1]));
    }
  };
  rot(0, 1);
  u32 vtg[2];
  u64 epoch = 0;
  auto mop = [&](auto ST, auto I, auto NOP) {
    constexpr int st = decltype(ST)::v, i = decltype(I)::v;
    constexpr bool nop = decltype(NOP)::v != 0;
    constexpr MList L = stage_ops(st);
    if constexpr (i < L.n) {
      constexpr int k = L.k[i], a = L.a[i], bb = L.b[i];
      auto m0 = [&](u32 v) { if constexpr (nop) SETM0N(v); else SETM0(v); };
      if constexpr (k == K_M0X) m0((u32)(a * 20480 + bb * 1024) + (u32)w * 5120u);
      if constexpr (k == K_X) DMAn<(bb & 3) * 1024>(voff_x[bb], srd_x, sX[a]);
      if constexpr (k == K_M0W) {
        if constexpr (a == 3 && bb == 1) m0(w < 2 ? 73728u + (u32)w * 3072u : L_SCR + 2048u + (u32)(w - 2) * 3072u);
        else m0(wreg(a) + (u32)(bb * 12288) + (u32)w * 3072u);
      }
      if constexpr (k == K_W) DMAn<(bb % 3) * 1024>((bb < 3 ? voff_w + 0u : voff_w1 + 0u), srd_qkv, sW[a]);
      if constexpr (k == K_M0G) m0((u32)gblock(a, w) * 4096u);
      if constexpr (k == K_G) DMAn<bb * 1024>(voff_g[bb], selsrd(gmat(a), srd_u, srd_g), (u32)(gblock(a, w) & 15) * (32u * 3584u));
      if constexpr (k == K_E) epoch = ld_flag(A.ws, WS_EPOCH);
      if constexpr (k == K_T) {
        if constexpr (a == 5) TOUCH(tv, vtg[0], selsrd((w & 1) != 0, srd_u, srd_g), 0u);
        if constexpr (a == 6) TOUCH(tv, vtg[1], selsrd(w < 2, srd_u, srd_g), 0u);
        if constexpr (a == 7) TOUCH(tv, (u32)((sl & 3) * 128 + w * 32 + (lane & 31)) * 1024u + (u32)ct * 128u, srd_o, 0u);
      }
    }
  };
  TS(39);
  sfor<0, stage_ops(-2).n>([&](auto I) { mop(IC<-2>{}, I, IC<1>{}); });
  SB();
  TS(40);
  rot(1, 4);
#pragma unroll
  for (int i = 0; i < 4; i++)
    voff_g[i] = (u32)(8 * i + r8) * 3584u - (u32)i * 1024u + (u32)fb * 128u + (u32)((((c16 >> 2) ^ ((r8 >> 1) & 1)) * 4 + (c16 & 3)) * 16);
#pragma unroll
  for (int a = 0; a < 2; a++) {
    const u32 bw = (u32)(lane >> 3) & 3u;
    u32 B = ((a ? 0x0400000cu : 0x080a0406u) >> (8 * w)) & 15u;
    B += bw + ((a == 1 && w == 3 && bw >= 2) ? 8u : 0u);
    vtg[a] = (B * 32u + (u32)((sl & 3) * 8 + (lane & 7))) * 3584u + (u32)fb * 128u;
  }
#pragma unroll
  for (int i = 0; i < 4; i++) OPQ(voff_g[i]);
  OPQ(vtg[0]); OPQ(vtg[1]);

  u32 xb[4], xb3[4], xsb[4];
#pragma unroll
  for (int a = 0; a < 4; a++) {
    xb[a] = (u32)(t16 >> 2) * 5120u + (u32)(t16 & 3) * 256u + (u32)((((((u32)a ^ (2u * rp)) << 2) | (u32)q4) ^ (u32)t16) << 4);
    xsb[a] = xb[a] + (u32)(w + 1) * 1024u;
    xb3[a] = xb[a] + 61440u;
    OPQ(xb[a]); OPQ(xsb[a]); OPQ(xb3[a]);
  }
  const u32 fR = ((lane >> 3) & 1) | (((lane >> 4) & 1) << 1);
  const u32 wlane = (u32)qp * 128u + (u32)(((u32)w ^ fR) * 32u) + (u32)(lane & 3) * 8u;
  u32 wbase = (u32)q4 * 3072u + wlane;
  u32 wbaseR = wbase + L_RING;
  u32 wbase7 = (q4 < 2 ? 73728u + (u32)q4 * 3072u : L_SCR + 2048u + (u32)(q4 - 2) * 3072u) + wlane;
  OPQ(wbase); OPQ(wbaseR); OPQ(wbase7);
  const u32 ONE2 = 0x3f803f80u;
  u32x4 ones = {ONE2, ONE2, ONE2, ONE2};
  OPQ(ones);

  f32x4 aq[4], ak[5], av[5], aqc, ag, as, ag0;
  const f32x4 z4 = {0.f, 0.f, 0.f, 0.f};

  auto rd1 = [&](auto KS, auto I, Frag& F) {
    constexpr int ks = decltype(KS)::v, i = decltype(I)::v;
    constexpr u32 xo = (ks >> 2) * 20480;
    constexpr u32 wa = ks == 7 ? 0u : wreg(ks >> 1) + (ks & 1) * 12288;
    constexpr u32 wo = wa >= L_RING ? wa - L_RING : wa;
    const u32 wb = ks == 7 ? wbase7 + 0u : wa >= L_RING ? wbaseR + 0u : wbase + 0u;
    const u32 xq = ks >= 12 ? xb3[ks & 3] + 0u : xb[ks & 3] + xo;
    if constexpr (i == 0) F.wq[0] = lds_tr(wb + wo);
    if constexpr (i == 1) F.wq[1] = lds_tr(wb + wo + 512);
    if constexpr (i >= 2 && i <= 5) F.x[i - 1] = lds_r128(xq + (i - 1) * 1024);
    if constexpr (i == 6) F.wk[0] = lds_tr(wb + wo + 1024);
    if constexpr (i == 7) F.wk[1] = lds_tr(wb + wo + 1024 + 512);
    if constexpr (i == 8) F.wv[0] = lds_tr(wb + wo + 2048);
    if constexpr (i == 9) F.wv[1] = lds_tr(wb + wo + 2048 + 512);
    if constexpr (i == 10) F.x[0] = lds_r128(xq);
    if constexpr (i == 11) F.xs = lds_r128(xsb[ks & 3] + xo);
  };
  auto mm1 = [&](auto I, Frag& F, auto FIRST) {
    constexpr int i = decltype(I)::v;
    constexpr bool in = decltype(FIRST)::v != 0;
    u32x4 wq = cat(F.wq[0], F.wq[1]), wk = cat(F.wk[0], F.wk[1]), wv = cat(F.wv[0], F.wv[1]);
    if constexpr (i < 4) MMA16<in>(aq[i], wq, F.x[i + 1]);
    else if constexpr (i < 8) MMA16<in>(ak[i - 3], wk, F.x[i - 3]);
    else if constexpr (i < 12) MMA16<in>(av[i - 7], F.x[i - 7], wv);
    else if constexpr (i == 12) MMA16<in>(aqc, wq, F.x[0]);
    else if constexpr (i == 13) MMA16<in>(ak[0], wk, F.x[0]);
    else if constexpr (i == 14) MMA16<in>(av[0], F.x[0], wv);
    else if constexpr (i == 15) MMA16<in>(ag0, F.x[0], F.x[0]);
    else if constexpr (i == 16) MMA16<in>(ag, F.xs, F.xs);
    else MMA16<in>(as, ones, F.xs);
  };
  u32 sta0 = q4 == qp ? L_SCR + (u32)t16 * 8u : L_SCR + 1024u + (u32)lane * 8u;
  u32 sta1 = q4 == qp ? L_SCR + (u32)(16 * (w + 1) + t16) * 8u : L_SCR + 1024u + (u32)lane * 8u;
  OPQ(sta0); OPQ(sta1);

  SB();
  sfor<0, stage_ops(-1).n>([&](auto I) { mop(IC<-1>{}, I, IC<1>{}); });
  TS(37);
  Frag FA, FB;
  sfor<0, 8>([&](auto ST) {
    constexpr int st = decltype(ST)::v;
    constexpr MList L = stage_ops(st);
    static_assert(gap_first(st, 36, L.n) >= L.n, "not enough slots");
    WAIT_VM(vm_allow(st));
    if constexpr ((st & 1) == 0) lds_w128((u32)((st >> 1) * 20480) + (u32)t16 * 16u, ones);
    BAR();
    if constexpr (st == 0) TS(1);
    if constexpr (st > 0) TS(16 + st);
    auto vm = [&](auto G) {
      constexpr int g = decltype(G)::v, f = gap_first(st, g, L.n), c = gap_cap(st, g, L.n);
      constexpr bool bare = st == 0 && g < 18;
      if constexpr (c >= 1) mop(ST, IC<f>{}, IC<(c == 2 || bare) ? 1 : 0>{});
      if constexpr (c >= 2) mop(ST, IC<f + 1>{}, IC<0>{});
    };
    sfor<0, 18>([&](auto I) {
      constexpr int i = decltype(I)::v;
      if constexpr (i < 12) rd1(IC<2 * st>{}, I, FA);
      if constexpr (st > 0) mm1(I, FB, IC<0>{});
      SB();
      vm(I);
      SB();
    });
    sfor<0, 18>([&](auto I) {
      constexpr int i = decltype(I)::v;
      mm1(I, FA, IC<st == 0 ? 1 : 0>{});
      if constexpr (i < 12) rd1(IC<2 * st + 1>{}, I, FB);
      SB();
      vm(IC<18 + i>{});
      SB();
    });
  });
  {
    const bool l1 = (lane & 1) != 0, l2 = (lane & 2) != 0;
    float sa, sb, s0, ma, mb;
    sfor<0, 18>([&](auto I) {
      constexpr int i = decltype(I)::v;
      mm1(IC<17 - i>{}, FB, IC<0>{});
      SB();
      if constexpr (i == 9) { sa = l1 ? ag[1] : ag[0]; sb = l1 ? ag[3] : ag[2]; }
      if constexpr (i == 10) { sa = l2 ? sb : sa; ma = as[0] * (1.f / 512.f); }
      if constexpr (i == 11) { sb = l1 ? ag0[1] : ag0[0]; s0 = l1 ? ag0[3] : ag0[2]; }
      if constexpr (i == 12) { sb = l2 ? s0 : sb; sa *= (1.f / 512.f); }
      if constexpr (i == 13) { s0 = bcast_row0(ag0[0]); }
      if constexpr (i == 14) { sa = fmaxf(__builtin_fmaf(-ma, ma, sa), 0.f); }
      if constexpr (i == 15) { mb = s0 * (1.f / 512.f); sb *= (1.f / 512.f); }
      if constexpr (i == 16) { sa = __builtin_amdgcn_rsqf(sa + 1e-5f); }
      if constexpr (i == 17) { sb = fmaxf(__builtin_fmaf(-mb, mb, sb), 0.f); }
      if constexpr (i == 9) OPQ(as);
      if constexpr (i >= 9) { OPQ(sa); OPQ(sb); }
      if constexpr (i >= 10) OPQ(ma);
      if constexpr (i >= 11 && i < 15) OPQ(s0);
      if constexpr (i >= 15) OPQ(mb);
      SB();
    });
    TS(2);
    lds_w64(sta1, u32x2{asu(sa), asu(-ma * sa)});
    sb = __builtin_amdgcn_rsqf(sb + 1e-5f);
    lds_w64(sta0, u32x2{asu(sb), asu(-mb * sb)});
  }

  BAR();
  TS(41);
  auto gslot = [&](auto S) {
    constexpr int s = decltype(S)::v;
    SETM0N((u32)gblock(s, w) * 4096u);
    sfor<0, 4>([&](auto I) {
      constexpr int i = decltype(I)::v;
      DMAn<i * 1024>(voff_g[i], selsrd(gmat(s), srd_u, srd_g), (u32)(gblock(s, w) & 15) * (32u * 3584u));
    });
  };
  auto glate = [&](auto N) {
    constexpr int n = decltype(N)::v;
    sfor<0, 8>([&](auto S) {
      constexpr int s = decltype(S)::v;
      constexpr int idx = [] { int c = 0; for (int i = 0; i < s; i++) c += gstage(i) == 8; return c; }();
      if constexpr (gstage(s) == 8 && idx == n) gslot(S);
    });
    SB();
  };
  glate(IC<0>{}); glate(IC<1>{});
  SB();

  const rsrc_t rs_o = mkrs(A.wo, 512u * 1024u);
  u32x4 tb2[4][4];
  auto b2_load = [&](auto J) {
    constexpr int j = decltype(J)::v;
#pragma unroll
    for (int i = 0; i < 4; i++)
      tb2[j][i] = __builtin_amdgcn_raw_buffer_load_b128(rs_o, (u32)r8 * 1024u + (u32)c16 * 16u, (u32)((128 * w + 32 * j + 8 * i) * 1024 + ct * 128), 0);
    SB();
  };

  f32x4 csq, csk;
#pragma unroll
  for (int i = 0; i < 4; i++) {
    csq[i] = asf((u32)__builtin_amdgcn_update_dpp(0, (int)asu(aqc[i]), 0x150, 0xf, 0xf, false));
    csk[i] = asf((u32)__builtin_amdgcn_update_dpp(0, (int)asu(ak[0][i]), 0x150, 0xf, 0xf, false));
  }
  float csv = bcast_row0(av[0][0]);
  const float QS = 0.25f * 1.4426950408889634f;
  u32x2 qf[4], kf[5], vf[5];
#pragma unroll
  for (int T = 0; T < 5; T++) {
    u32x2 st = lds_r64(L_SCR + (u32)(16 * T + t16) * 8u);
    float rs = asf(st.x), nm = asf(st.y);
    f32x4 kk = ak[T] * rs + csk * nm;
    kf[T] = u32x2{pk2(kk[0], kk[1]), pk2(kk[2], kk[3])};
    if (T > 0) {
      f32x4 qq = aq[T - 1] * (rs * QS) + csq * (nm * QS);
      qf[T - 1] = u32x2{pk2(qq[0], qq[1]), pk2(qq[2], qq[3])};
    }
  }
  SB();
  glate(IC<2>{});
  const float NEG = -1e30f;
  f32x4 bC, bP, bP1;
#pragma unroll
  for (int i = 0; i < 4; i++) {
    int kap = 4 * q4 + i;
    bool par = ((t16 - kap) & 1) == 0;
    bC[i] = (par && kap <= t16) ? 0.f : NEG;
    bP[i] = (par && kap >= t16 + 2) ? 0.f : NEG;
    bP1[i] = rg == 0 ? NEG : bP[i];
  }
  f32x4 sp[4], sc[4];
#pragma unroll
  for (int j = 0; j < 4; j++) {
    sp[j] = mma16k16(kf[j], qf[j], j == 0 ? bP1 : bP);
    sc[j] = mma16k16(kf[j + 1], qf[j], bC);
  }
#pragma unroll
  for (int T = 0; T < 5; T++) {
    u32x4 sa = lds_r128(L_SCR + (u32)(16 * T + 4 * q4) * 8u);
    u32x4 sb = lds_r128(L_SCR + (u32)(16 * T + 4 * q4) * 8u + 16u);
    float v0 = av[T][0] * asf(sa.x) + csv * asf(sa.y);
    float v1 = av[T][1] * asf(sa.z) + csv * asf(sa.w);
    float v2 = av[T][2] * asf(sb.x) + csv * asf(sb.y);
    float v3 = av[T][3] * asf(sb.z) + csv * asf(sb.w);
    vf[T] = u32x2{pk2(v0, v1), pk2(v2, v3)};
  }
  SB();
  glate(IC<3>{});
  const u32 half = (t16 & 1) ? 0xffff0000u : 0x0000ffffu;
  u32 mC[2], mP[2];
#pragma unroll
  for (int a = 0; a < 2; a++) {
    bool isc = (4 * q4 + 2 * a + (t16 & 1)) <= t16;
    mC[a] = isc ? half : 0u; mP[a] = isc ? 0u : half;
  }
  float e0[4], e1[4], mx[4];
#pragma unroll
  for (int j = 0; j < 4; j++) {
    e0[j] = fmaxf(fmaxf(sp[j][0], sp[j][1]), fmaxf(sc[j][0], sc[j][1]));
    e1[j] = fmaxf(fmaxf(sp[j][2], sp[j][3]), fmaxf(sc[j][2], sc[j][3]));
    mx[j] = fmaxf(e0[j], e1[j]);
  }
  {
    auto s01 = __builtin_amdgcn_permlane16_swap(asu(mx[0]), asu(mx[1]), false, false);
    auto s23 = __builtin_amdgcn_permlane16_swap(asu(mx[2]), asu(mx[3]), false, false);
    float ab = fmaxf(asf(s01[0]), asf(s01[1]));
    float cd = fmaxf(asf(s23[0]), asf(s23[1]));
    auto s2 = __builtin_amdgcn_permlane32_swap(asu(ab), asu(cd), false, false);
    float R = fmaxf(asf(s2[0]), asf(s2[1]));
    auto r2 = __builtin_amdgcn_permlane32_swap(asu(R), asu(R), false, false);
    auto ra = __builtin_amdgcn_permlane16_swap(r2[0], r2[0], false, false);
    auto rc = __builtin_amdgcn_permlane16_swap(r2[1], r2[1], false, false);
    mx[0] = asf(ra[0]); mx[1] = asf(ra[1]); mx[2] = asf(rc[0]); mx[3] = asf(rc[1]);
  }
  u32x2 ob[4];
#pragma unroll
  for (int j = 0; j < 4; j++) {
    float p0 = __builtin_amdgcn_exp2f(e0[j] - mx[j]);
    float p1 = __builtin_amdgcn_exp2f(e1[j] - mx[j]);
    u32 k0 = pk2(p0, p0), k1 = pk2(p1, p1);
    u32x4 P = {k0 & mP[0], k1 & mP[1], k0 & mC[0], k1 & mC[1]};
    f32x4 o = mma16(cat(vf[j], vf[j + 1]), P, z4);
    f32x4 sm = mma16(ones, P, z4);
    float inv = __builtin_amdgcn_rcpf(sm[0]);
    o = o * inv;
    ob[j] = u32x2{pk2(o[0], o[1]), pk2(o[2], o[3])};
  }
  const rsrc_t rs_ws = mkrs(A.ws, WS_SIZE);
  const int head = 4 * hq + w;
  const u32 oo = WS_OBUF + (u32)((2 * rg) * 32 + head) * 1024u;
  __builtin_amdgcn_raw_buffer_store_b128(cat(ob[0], ob[2]), rs_ws, lane * 16, oo, SC1);
  __builtin_amdgcn_raw_buffer_store_b128(cat(ob[1], ob[3]), rs_ws, lane * 16, oo + 32768u, SC1);
  SB();
  TS(3);
  glate(IC<4>{});
  WAIT_VM(4);
  asm volatile("" ::"v"(tv));
  const u64 tag = epoch + 0x9E3779B97F4A7C15ull;
  if (lane == 0) st_flag(A.ws, WS_F1 + (u32)(rg * 32 + head) * 8u, tag);
  TS(4);
  b2_load(IC<0>{}); b2_load(IC<1>{}); b2_load(IC<2>{}); b2_load(IC<3>{});
  WAIT_VM(16);
  if (!p3) { run_tail<false>(A, lane, w, xcd, sl, tag, tb2); __builtin_amdgcn_endpgm(); }
  run_tail<true>(A, lane, w, xcd, sl, tag, tb2);
}

template <bool DO_P3>
DEVI void run_tail(const Args& A, const int lane, const int w, const int xcd, const int sl, const u64 tag,
                   u32x4 (&tb2)[4][4]
) {
  const int t16 = lane & 15, q4 = lane >> 4, r8 = lane >> 3, c16 = lane & 7, qp = (lane & 15) >> 2;
  const int rt = 4 * xcd + (sl & 3), ct = sl >> 2;
  const rsrc_t rs_ws = mkrs(A.ws, WS_SIZE);
  const rsrc_t rs_x = mkrs(A.x, 2048u * 1024u);
  const rsrc_t rs_d = mkrs(A.wd, 1792u * 1024u);
  const rsrc_t rs_out = mkrs(A.out, 2048u * 1024u);
  const f32x4 z4 = {0.f, 0.f, 0.f, 0.f};
  const int utt = w >> 1, uG = w & 1;
  const u32 ONE2 = 0x3f803f80u;

  const u32 f2 = ((q4 & 1) << 1) | (qp >> 1);
  const u32 ring = L_TOP + (u32)w * 8192u;
  const u32 voff_d = (u32)r8 * 1024u + (u32)c16 * 16u;
  const u32 wr_d = ring + (u32)r8 * 128u + (u32)((((c16 >> 1) ^ ((r8 >> 1) & 3)) * 2 + (c16 & 1)) * 16);
  u32 b4c[4];
#pragma unroll
  for (int c = 0; c < 4; c++)
    b4c[c] = ring + (u32)(16 * (q4 >> 1) + 4 * (q4 & 1) + qp) * 128u + (u32)(((u32)(c ^ (2 * uG)) ^ f2) * 32u) + (u32)(lane & 3) * 8u;
  u32x2 B4[12][4][2];
  u32x4 tmp[14][4];
  auto b4_load1 = [&](auto J, auto I) {
    constexpr int j = decltype(J)::v, i = decltype(I)::v;
    tmp[j][i] = __builtin_amdgcn_raw_buffer_load_b128(rs_d, voff_d, (u32)((448 * w + 32 * j + 8 * i) * 1024 + ct * 128), 0);
  };
  auto b4_load = [&](auto J) { sfor<0, 4>([&](auto I) { b4_load1(J, I); }); };
  auto b4_op = [&](auto J, auto O) {
    constexpr int j = decltype(J)::v, o = decltype(O)::v;
    constexpr u32 so = j == 12 ? 0u : 4096u;
    if constexpr (o < 4) lds_w128(wr_d + so + o * 1024, tmp[j][o]);
    else if constexpr (j < 12) B4[j][(o - 4) >> 1][o & 1] = lds_tr(b4c[(o - 4) >> 1] + so + (o & 1) * 1024);
  };

  u32 tv = 0;
  const u32x4 srd_d = mksrd(A.wd, 1792u * 1024u);
  if constexpr (NBW1 > 0) TOUCH(tv, (u32)(448 * (sl & 3) + w * (8 * NBW1) + (lane < 8 * NBW1 ? lane : 0)) * 1024u + (u32)ct * 128u, srd_d, 0u);
  u32x2 xr[2][2];
#pragma unroll
  for (int s = 0; s < 2; s++)
#pragma unroll
    for (int c = 0; c < 2; c++)
      xr[s][c] = __builtin_amdgcn_raw_buffer_load_b64(rs_x, (u32)t16 * 1024u + (u32)q4 * 8u + (u32)c * 32u,
                                                     (u32)((64 * rt + 32 * s + 16 * utt) * 1024 + ct * 128 + uG * 64), 0);
  SB();
  float cs[4];
  if constexpr (DO_P3) {
    const int fi = q4 & 1, kh = q4 >> 1;
    const u32 tb = (u32)(4 * kh + qp) * 128u + (u32)((qp >> 1) * 64) + (u32)fi * 32u + (u32)(lane & 3) * 8u;
    u32x4 sel[4];
#pragma unroll
    for (int e = 0; e < 4; e++) {
      u32 v = ((t16 & 3) == e && ((t16 >> 2) & 1) == (q4 & 1)) ? ONE2 : 0u;
      sel[e] = u32x4{v, v, v, v};
      OPQ(sel[e]);
    }
    f32x4 ca = z4;
    u32x4 f[2][4];
    auto rd = [&](auto S) {
      constexpr int s = decltype(S)::v;
      const u32 reg = (u32)gblock(s, w) * 4096u;
      const u32 r0 = tb + reg, r1 = (tb ^ 64u) + reg;
#pragma unroll
      for (int e = 0; e < 2; e++) {
        f[s & 1][2 * e] = cat(lds_tr(r0 + e * 1024), lds_tr(r0 + e * 1024 + 2048));
        f[s & 1][2 * e + 1] = cat(lds_tr(r1 + e * 1024), lds_tr(r1 + e * 1024 + 2048));
      }
    };
    rd(IC<0>{});
    sfor<0, 8>([&](auto S) {
      constexpr int s = decltype(S)::v;
      if constexpr (s < 7) rd(IC<s + 1>{});
      if constexpr (s < NBW1) b4_load(S);
      SB();
      const u32 wa = (u32)lane * 16u + (u32)gblock(s, w) * 4096u;
#pragma unroll
      for (int q = 0; q < 4; q++) {
        lds_w128(wa + q * 1024, f[s & 1][q]);
        ca = mma16(sel[2 * gmat(s) + (q & 1)], f[s & 1][q], ca);
      }
      SB();
    });
    sfor<8, (NBW1 > 8 ? NBW1 : 8)>([&](auto J) { b4_load(J); });
    if (lane < 32) lds_w128(ring + 4096u + (u32)lane * 16u, __builtin_bit_cast(u32x4, ca));
  } else {
    sfor<0, NBW1>([&](auto J) { b4_load(J); });
  }
  TS(44);
  BAR();
  if constexpr (DO_P3) {
    f32x4 c4 = z4;
#pragma unroll
    for (int m = 0; m < 4; m++) c4 += __builtin_bit_cast(f32x4, lds_r128(L_TOP + (u32)m * 8192u + 4096u + (u32)(lane & 31) * 16u));
    cs[0] = c4[0]; cs[1] = c4[1]; cs[2] = c4[2]; cs[3] = c4[3];
  }
  u32x4 B2[4][4];
  {
    u32 b2c[4];
#pragma unroll
    for (int c = 0; c < 4; c++)
      b2c[c] = ring + (u32)(4 * q4 + qp) * 128u + (u32)(((u32)(c ^ (2 * uG)) ^ f2) * 32u) + (u32)(lane & 3) * 8u;
#pragma unroll
    for (int ks = 0; ks < 4; ks++) {
#pragma unroll
      for (int o = 0; o < 4; o++) lds_w128(wr_d + o * 1024, tb2[ks][o]);
#pragma unroll
      for (int c = 0; c < 4; c++) B2[ks][c] = cat(lds_tr(b2c[c]), lds_tr(b2c[c] + 2048));
      SB();
    }
  }
  BAR();
  TS(7);

  u32x4 hres[2];
  {
    poll_flags(A.ws, WS_F1 + (u32)(rt * 32 + 8 * w) * 8u, lane, 8, tag);
    TS(5);
    u32x4 ol[2][8];
#pragma unroll
    for (int t = 1; t >= 0; t--)
#pragma unroll
      for (int i = 0; i < 8; i++)
        ol[t][i] = __builtin_amdgcn_raw_buffer_load_b128(rs_ws, lane * 16 + (i & 3) * 1024, WS_OBUF + (u32)((2 * rt + (t ^ utt)) * 32 + 8 * w + (i & 4)) * 1024u, SC1);
    SB();
    f32x4 acc[2][2][4];
    auto gemm = [&](auto T) {
      constexpr int t = decltype(T)::v;
#pragma unroll
      for (int ks = 0; ks < 4; ks++) {
        u32x4 La = ol[t][2 * ks], Lb = ol[t][2 * ks + 1];
        u32x4 b0 = {La.x, La.y, Lb.x, Lb.y}, b1 = {La.z, La.w, Lb.z, Lb.w};
#pragma unroll
        for (int c = 0; c < 4; c++) {
          acc[t][0][c] = mma16(B2[ks][c], b0, ks == 0 ? z4 : acc[t][0][c]);
          acc[t][1][c] = mma16(B2[ks][c], b1, ks == 0 ? z4 : acc[t][1][c]);
        }
      }
    };
    const u32 own = ring + (u32)lane * 16u;
    const u32 pa = L_TOP + (u32)(w ^ 2) * 8192u + (u32)lane * 16u;
    const u32 pb = L_TOP + (u32)(w ^ 1) * 8192u + (u32)lane * 16u;
    gemm(IC<1>{});
    SB();
    gemm(IC<0>{});
#pragma unroll
    for (int s = 0; s < 2; s++)
#pragma unroll
      for (int c = 0; c < 4; c++) lds_w128(pa + (s * 4 + c) * 1024, __builtin_bit_cast(u32x4, acc[1][s][c]));
#pragma unroll
    for (int i = 0; i < 8; i++) {
      __builtin_amdgcn_sched_group_barrier(0x008, 4, 0);
      __builtin_amdgcn_sched_group_barrier(0x200, 1, 0);
    }
    SB();
    TS(42);
    BAR();
#pragma unroll
    for (int c = 3; c >= 0; c--)
#pragma unroll
      for (int s = 0; s < 2; s++) acc[0][s][c] += __builtin_bit_cast(f32x4, lds_r128(own + (s * 4 + c) * 1024));
    WAIT_LGKM0();
#pragma unroll
    for (int s = 0; s < 2; s++)
#pragma unroll
      for (int c = 0; c < 2; c++) lds_w128(own + (s * 2 + c) * 1024, __builtin_bit_cast(u32x4, acc[0][s][2 + c]));
#pragma unroll
    for (int s = 0; s < 2; s++)
#pragma unroll
      for (int c = 0; c < 2; c++)
        acc[0][s][c] += f32x4{bflo(xr[s][c].x), bfhi(xr[s][c].x), bflo(xr[s][c].y), bfhi(xr[s][c].y)};
    BAR();
    float y[2];
#pragma unroll
    for (int s = 0; s < 2; s++) {
      f32x4 r0 = acc[0][s][0] + __builtin_bit_cast(f32x4, lds_r128(pb + (s * 2) * 1024));
      f32x4 r1 = acc[0][s][1] + __builtin_bit_cast(f32x4, lds_r128(pb + (s * 2 + 1) * 1024));
      u32x4 h = {pk2(r0[0], r0[1]), pk2(r0[2], r0[3]), pk2(r1[0], r1[1]), pk2(r1[2], r1[3])};
      __builtin_amdgcn_raw_buffer_store_b128(h, rs_ws, (u32)(q4 >> 1) * 1024u + (u32)((q4 & 1) * 32 + t16) * 16u,
                                             WS_HBUF + (u32)((2 * rt + s) * 32 + 2 * (2 * ct + uG)) * 1024u + (u32)utt * 256u, SC1);
      float sm = 0.f, sq = 0.f;
#pragma unroll
      for (int i = 0; i < 4; i++) {
        float a = bflo(h[i]), c = bfhi(h[i]);
        sm += a + c; sq += a * a + c * c;
      }
      hres[s] = h;
      OPQ(hres[s]);
      auto x1 = __builtin_amdgcn_permlane16_swap(asu(sm), asu(sq), false, false);
      y[s] = asf(x1[0]) + asf(x1[1]);
    }
    auto x2 = __builtin_amdgcn_permlane32_swap(asu(y[0]), asu(y[1]), false, false);
    float zz = asf(x2[0]) + asf(x2[1]);
    __builtin_amdgcn_raw_buffer_store_b32(asu(zz), rs_ws, (u32)t16 * 16u + (u32)q4 * 4u,
                                          WS_HSTAT + (u32)((rt * 16 + 2 * ct + uG) * 32 + 16 * utt) * 16u, SC1);
    SB();
    TS(43);
    sfor<0, 2>([&](auto J) { sfor<0, 12>([&](auto O) { b4_op(J, O); }); });
    SB();
    WAIT_VM(0);
    asm volatile("" ::"v"(tv));
    if (lane == 0) st_flag(A.ws, WS_F2 + (u32)(rt * 32 + ct * 4 + w) * 8u, tag);
    sfor<NBW1, NBP>([&](auto J) { b4_load(J); });
    BAR();
  }
  TS(6);

  u32x4 ml[14][2];
  auto m_load1 = [&](auto S, auto J, auto TT) {
    constexpr int s = decltype(S)::v, j = decltype(J)::v, tt = decltype(TT)::v;
    ml[j][tt] = __builtin_amdgcn_raw_buffer_load_b128(rs_ws, lane * 16 + (j & 3) * 1024,
                                                     WS_MBUF + (u32)(((2 * rt + s) * 2 + (tt ^ utt)) * 56 + 14 * w + (j & ~3)) * 1024u, SC1);
  };
  auto m_load = [&](auto S, auto J) { m_load1(S, J, IC<0>{}); m_load1(S, J, IC<1>{}); };
  const u32 f3p = WS_F3 + (u32)(rt * 32 + 7 * w) * 8u;
  u32 f3o = 0;

  if constexpr (DO_P3) {
    const int rg3 = 4 * (xcd >> 2) + (sl & 3), fb = 7 * (xcd & 3) + (sl >> 2);
    sfor<2, NBP>([&](auto J) { sfor<0, 12>([&](auto O) { b4_op(J, O); }); SB(); });
    TS(9);

    const int r64 = 4 * rg3 + w, r32a = 2 * r64;
    f3o = WS_F3 + (u32)(r64 * 32 + fb) * 8u;
    poll_flags(A.ws, WS_F2 + (u32)(r64 * 32) * 8u, lane, 32, tag);
    TS(8);
    u32x4 hx[32];
    float ek1[2], ek2[2], ev[2];
    u32x4 mfr[2];
    u32x4 stl[8];
    auto st_load = [&]() {
#pragma unroll
      for (int i = 0; i < 8; i++)
        stl[i] = __builtin_amdgcn_raw_buffer_load_b128(rs_ws, (u32)(lane & 31) * 16u + (u32)(lane >> 5) * 4096u + (u32)i * 512u,
                                                      WS_HSTAT + (u32)(r64 * 16 * 32) * 16u, SC1);
    };
    auto st_fin = [&](auto S) {
      constexpr int s = decltype(S)::v;
      float sm = 0.f, sq = 0.f;
#pragma unroll
      for (int i = 0; i < 8; i++) { sm += asf(stl[i][2 * s]); sq += asf(stl[i][2 * s + 1]); }
      auto a = __builtin_amdgcn_permlane32_swap(asu(sm), asu(sm), false, false);
      auto c = __builtin_amdgcn_permlane32_swap(asu(sq), asu(sq), false, false);
      sm = asf(a[0]) + asf(a[1]); sq = asf(c[0]) + asf(c[1]);
      float mean = sm * (1.f / 512.f);
      float var = fmaxf(sq * (1.f / 512.f) - mean * mean, 0.f) + 1e-5f;
      float rstd = __builtin_amdgcn_rsqf(var);
      const float C1 = -2.f * 0.7978845608028654f * 1.4426950408889634f;
      const float C2 = C1 * 0.044715f;
      ek1[s] = C1 * rstd; ek2[s] = C2 * rstd * rstd * rstd; ev[s] = var;
      float m = -mean;
      u32 hi = pk2(m, m); float r1 = m - bflo(hi);
      u32 mi = pk2(r1, r1); float r2 = r1 - bflo(mi);
      u32 lo = pk2(r2, r2);
      u32x4 f = {(hi & 0xffffu) | (mi & 0xffff0000u), (lo & 0xffffu) | (hi & 0xffff0000u), (mi & 0xffffu) | (hi & 0xffff0000u), 0u};
      if (lane >= 32) f = u32x4{0u, 0u, 0u, 0u};
      mfr[s] = f;
      OPQ(mfr[s]); OPQ(ek1[s]); OPQ(ek2[s]); OPQ(ev[s]);
    };
    auto cs_frag = [&](float c) {
      u32 hi = pk2(c, c); float r1 = c - bflo(hi);
      u32 mi = pk2(r1, r1); float r2 = r1 - bflo(mi);
      u32 lo = pk2(r2, r2);
      u32x4 f = u32x4{hi, (hi & 0xffffu) | (mi & 0xffff0000u), (mi & 0xffffu) | (lo & 0xffff0000u), 0u};
      OPQ(f);
      return f;
    };
    auto h_load = [&](auto S, auto K) {
      constexpr int s = decltype(S)::v, k = decltype(K)::v;
      hx[k] = __builtin_amdgcn_raw_buffer_load_b128(rs_ws, lane * 16 + (k & 3) * 1024, WS_HBUF + (u32)((r32a + s) * 32 + (k & ~3)) * 1024u, SC1);
    };
    constexpr int NH0 = 12;
    sfor<0, NH0>([&](auto K) { h_load(IC<0>{}, K); });
    SB();
    u32x4 csf[2];

    f32x16 ag[2], au[2];
    u32 mo[8];
    float ea[2], eb[2];
    auto epi = [&](auto P, auto E, auto Q) {
      constexpr int p = decltype(P)::v, e = decltype(E)::v, q = decltype(Q)::v;
      constexpr int a = p & 1, s = p >> 1;
      const float g0 = ag[a][2 * e], g1 = ag[a][2 * e + 1];
      if constexpr (q == 0) { ea[0] = g0 * g0; ea[1] = g1 * g1; ea[0] = __builtin_fmaf(ea[0], ek2[s], ek1[s]); }
      if constexpr (q == 1) { ea[1] = __builtin_fmaf(ea[1], ek2[s], ek1[s]); ea[0] *= g0; ea[1] *= g1; }
      if constexpr (q == 2) { ea[0] = __builtin_amdgcn_exp2f(ea[0]); eb[0] = g0 * au[a][2 * e]; }
      if constexpr (q == 3) { ea[1] = __builtin_amdgcn_exp2f(ea[1]); eb[1] = g1 * au[a][2 * e + 1]; }
      if constexpr (q == 4) { ea[0] = __builtin_amdgcn_rcpf(__builtin_fmaf(ea[0], ev[s], ev[s])); }
      if constexpr (q == 5) { ea[1] = __builtin_amdgcn_rcpf(__builtin_fmaf(ea[1], ev[s], ev[s])); }
      if constexpr (q == 6) { mo[e] = pk2(eb[0] * ea[0], eb[1] * ea[1]); }
      OPQ(ea[0]); OPQ(ea[1]); OPQ(eb[0]); OPQ(eb[1]);
    };
    const u32 mvo = (u32)((lane >> 4) & 1) * (56u * 1024u) + (u32)((lane >> 5) * 16 + t16) * 16u;
    auto epi_store = [&](auto P, auto AH) {
      constexpr int p = decltype(P)::v, ah = decltype(AH)::v;
      constexpr int fh = p & 1, s = p >> 1;
      u32x4 v = {mo[4 * ah], mo[4 * ah + 1], mo[4 * ah + 2], mo[4 * ah + 3]};
      __builtin_amdgcn_raw_buffer_store_b128(v, rs_ws, mvo + (u32)(fh * 1024 + ah * 512),
                                             WS_MBUF + (u32)((r32a + s) * 2 * 56 + 2 * fb) * 1024u, SC1);
    };
    ea[0] = ea[1] = eb[0] = eb[1] = 0.f;

    u32x2 wbc;
    auto wfrag = [&](auto GI) {
      constexpr int G = decltype(GI)::v, fh = (G >> 6) & 1, k16 = (G >> 1) & 31, mat = G & 1;
      constexpr u32 reg = (k16 >> 1) * 4096u + (2 * (k16 & 1) + fh) * 1024u;
      if constexpr ((G & 63) == 0) { wbc = u32x2{(u32)lane * 16u, (u32)lane * 16u + 65536u}; OPQ(wbc); }
      return lds_r128((mat ? wbc.y : wbc.x) + reg);
    };
    constexpr int WD = 4;
    u32x4 wf[WD + 1];
    sfor<0, WD>([&](auto I) { wf[decltype(I)::v] = wfrag(I); });
    u64 fl1 = 0;
    constexpr int NREM = 14 - NBP;
    sfor<0, 256>([&](auto GI) {
      constexpr int G = decltype(GI)::v;
      constexpr int p = G >> 6, g = G & 63, k = g >> 1, mat = g & 1;
      constexpr int a = p & 1, fh = p & 1, s = p >> 1;
      if constexpr (G + WD < 256) wf[(G + WD) % (WD + 1)] = wfrag(IC<G + WD>{});
      {
        f32x16& acc = mat == 0 ? ag[a] : au[a];
        if constexpr (k == 0) MMA32_INIT(acc, wf[G % (WD + 1)], hx[k]);
        else if constexpr (k & 1) MMA32_ACC_A(acc, wf[G % (WD + 1)], hx[k]);
        else MMA32_ACC(acc, wf[G % (WD + 1)], hx[k]);
      }
      if constexpr (g == 62) MMA32_ACC(ag[a], csf[0], mfr[s]);
      if constexpr (g == 63) MMA32_ACC(au[a], csf[1], mfr[s]);
      if constexpr (g == 1) csf[0] = cs_frag(cs[fh]);
      if constexpr (g == 3) csf[1] = cs_frag(cs[2 + fh]);
      if constexpr (p > 0) {
        constexpr int E0 = 6;
        if constexpr (g >= E0 && g < E0 + 56) epi(IC<p - 1>{}, IC<(g - E0) / 7>{}, IC<(g - E0) % 7>{});
        if constexpr (g == E0 + 28) epi_store(IC<p - 1>{}, IC<0>{});
        if constexpr (g == E0 + 56) epi_store(IC<p - 1>{}, IC<1>{});
      }
      if constexpr (p == 0) {
        if constexpr (NH0 + g < 32) h_load(IC<0>{}, IC<NH0 + g>{});
        if constexpr (g == 32 - NH0) st_load();
        if constexpr (g == 34 - NH0 && NREM > 0)
          TOUCH(tv, (u32)(448 * (sl & 3) + 32 * NBP + w * (8 * NREM) + (lane < 8 * NREM ? lane : 0)) * 1024u + (u32)ct * 128u, srd_d, 0u);
        if constexpr (g == 52) st_fin(IC<0>{});
        if constexpr (g == 56) st_fin(IC<1>{});
      }
      if constexpr (p == 1) {
        if constexpr (mat == 1) h_load(IC<1>{}, IC<k>{});
      }
      sfor<0, NREM>([&](auto I) {
        constexpr int i = decltype(I)::v;
        sfor<0, 4>([&](auto LL) { if constexpr (b4_lpos(i, decltype(LL)::v) == G) b4_load1(IC<NBP + i>{}, LL); });
        constexpr int o = G - b4_spos(i);
        if constexpr (o >= 0 && o < (NBP + i < 12 ? 12 : 4)) b4_op(IC<NBP + i>{}, IC<o>{});
      });
      if constexpr (p == 3) {
        if constexpr (g == 20) {
          static_assert(NREM == 0 || b4_lpos(NREM - 1, 3) < 128 + 62, "");
          WAIT_VM(0);
          asm volatile("" ::"v"(tv));
          if (lane == 0) st_flag(A.ws, f3o, tag);
        }
        if constexpr (g == 40) fl1 = ld_flag(A.ws, f3p + (u32)(lane < 7 ? lane : 0) * 8u);
        if constexpr (g == 50) {
          if (!__all(fl1 == tag)) poll_flags(A.ws, f3p, lane, 7, tag);
          asm volatile("" ::: "memory");
        }
        if constexpr (g >= 50) m_load(IC<0>{}, IC<g - 50>{});
      }
      if constexpr ((g & 31) == 31) TS(24 + 2 * p + (g >> 5));
      SB();
    });
    TS(10);
    asm volatile("s_nop 11" ::: "memory");
    SB();
    sfor<0, 8>([&](auto E) {
      constexpr int e = decltype(E)::v;
      float y[2];
#pragma unroll
      for (int i = 0; i < 2; i++) {
        float g = ag[1][2 * e + i], u = au[1][2 * e + i];
        float ex = __builtin_amdgcn_exp2f(g * __builtin_fmaf(g * g, ek2[1], ek1[1]));
        y[i] = (g * u) * __builtin_amdgcn_rcpf(__builtin_fmaf(ex, ev[1], ev[1]));
      }
      mo[e] = pk2(y[0], y[1]);
      if constexpr (e == 3) epi_store(IC<3>{}, IC<0>{});
      if constexpr (e == 7) epi_store(IC<3>{}, IC<1>{});
    });
    SB();
    TS(45);
  } else {
    sfor<NBP, 14>([&](auto J) { b4_load(J); });
    SB();
    sfor<2, 14>([&](auto J) { sfor<0, 12>([&](auto O) { b4_op(J, O); }); });
    if (sl == 31 && xcd == 7) {
#pragma unroll 1
      for (int i = 0; i < 4; i++) {
        u32 o = WS_F1 + (u32)((w * 4 + i) * 64 + lane) * 8u;
        for (u32 it = 0; it < SPIN_MAX; ++it) {
          u64 f = ld_flag(A.ws, o);
          if (__all(f == tag)) break;
        }
      }
      BAR();
      if (w == 0 && lane == 0) st_flag(A.ws, WS_EPOCH, tag);
    }
    poll_flags(A.ws, f3p, lane, 7, tag);
    sfor<0, 14>([&](auto J) { m_load(IC<0>{}, J); });
    SB();
  }

  u32x4 bl[2][4];
#pragma unroll
  for (int j = 0; j < 2; j++)
#pragma unroll
    for (int c = 0; c < 4; c++) bl[j][c] = cat(lds_tr(b4c[c] + j * 4096), lds_tr(b4c[c] + j * 4096 + 1024));
  f32x4 acc[2][4];
  u32x4 mn[14][2];
  auto n_load = [&](auto J) {
    constexpr int j = decltype(J)::v;
#pragma unroll
    for (int tt = 0; tt < 2; tt++)
      mn[j][tt] = __builtin_amdgcn_raw_buffer_load_b128(rs_ws, lane * 16 + (j & 3) * 1024,
                                                       WS_MBUF + (u32)(((2 * rt + 1) * 2 + (tt ^ utt)) * 56 + 14 * w + (j & ~3)) * 1024u, SC1);
  };
  auto kstep = [&](auto J, const u32x4 (&m)[2], bool first) {
    constexpr int j = decltype(J)::v;
#pragma unroll
    for (int c = 0; c < 4; c++) {
      u32x4 bw;
      if constexpr (j < 12) bw = cat(B4[j][c][0], B4[j][c][1]); else bw = bl[j - 12][c];
      acc[0][c] = mma16(bw, m[0], first ? z4 : acc[0][c]);
      acc[1][c] = mma16(bw, m[1], first ? z4 : acc[1][c]);
    }
  };
  auto put = [&](auto S) {
    constexpr int s = decltype(S)::v;
#pragma unroll
    for (int u = 1; u < 4; u++)
#pragma unroll
      for (int c = 0; c < 2; c++)
        lds_w128((u32)s * 32768u + (u32)(w ^ u) * 8192u + (u32)lane * 16u + (u * 2 + c) * 1024, __builtin_bit_cast(u32x4, acc[u >> 1][2 * (u & 1) + c]));
  };
  auto fin = [&](auto S, const f32x4 (&mine)[2]) {
    constexpr int s = decltype(S)::v;
    const u32 ibr = (u32)s * 32768u + (u32)w * 8192u + (u32)lane * 16u;
    u32x2 o2[2];
#pragma unroll
    for (int c = 0; c < 2; c++) {
      u32 ha = hres[s][2 * c], hb = hres[s][2 * c + 1];
      f32x4 r = f32x4{bflo(ha), bfhi(ha), bflo(hb), bfhi(hb)} + mine[c];
      f32x4 p1 = __builtin_bit_cast(f32x4, lds_r128(ibr + (2 + c) * 1024));
      f32x4 p2 = __builtin_bit_cast(f32x4, lds_r128(ibr + (4 + c) * 1024));
      f32x4 p3 = __builtin_bit_cast(f32x4, lds_r128(ibr + (6 + c) * 1024));
      r += p1 + (p2 + p3);
      o2[c] = u32x2{pk2(r[0], r[1]), pk2(r[2], r[3])};
    }
    auto sx = __builtin_amdgcn_permlane16_swap(o2[0].x, o2[1].x, false, false);
    auto sy = __builtin_amdgcn_permlane16_swap(o2[0].y, o2[1].y, false, false);
    u32x4 ov = {sx[0], sy[0], sx[1], sy[1]};
    __builtin_amdgcn_raw_buffer_store_b128(ov, rs_out, (u32)t16 * 1024u + (u32)((q4 & 1) * 32 + (q4 >> 1) * 16),
                                           (u32)((64 * rt + 32 * s + 16 * utt) * 1024 + ct * 128 + uG * 64), 0);
  };
  u64 fl = 0;
  sfor<0, 14>([&](auto J) {
    constexpr int j = decltype(J)::v;
    kstep(J, ml[j], j == 0);
    SB();
    if constexpr (DO_P3 && j == 2) { WAIT_VM(2); if (lane == 0) st_flag(A.ws, f3o + 8192u, tag); }
    if constexpr (DO_P3 && j == 7) { WAIT_VM(1); if (lane == 0) st_flag(A.ws, f3o + 16384u, tag); }
    if constexpr (j == 8) fl = ld_flag(A.ws, f3p + 8192u + (u32)(lane < 7 ? lane : 0) * 8u);
    if constexpr (j == 12) {
      if (!__all(fl == tag)) poll_flags(A.ws, f3p + 8192u, lane, 7, tag);
      asm volatile("" ::: "memory");
      sfor<0, 7>([&](auto I) { n_load(IC<2 * decltype(I)::v>{}); });
      fl = ld_flag(A.ws, f3p + 16384u + (u32)(lane < 7 ? lane : 0) * 8u);
    }
    SB();
  });
  TS(11);
  BAR();
  put(IC<0>{});
  f32x4 keep[2] = {acc[0][0], acc[0][1]};
  BAR();
  fin(IC<0>{}, keep);
  SB();
  TS(12);
  if (!__all(fl == tag)) poll_flags(A.ws, f3p + 16384u, lane, 7, tag);
  asm volatile("" ::: "memory");
  TS(13);
  sfor<0, 7>([&](auto I) { n_load(IC<2 * decltype(I)::v + 1>{}); });
  SB();
  sfor<0, 7>([&](auto I) { constexpr int j = 2 * decltype(I)::v; kstep(IC<j>{}, mn[j], j == 0); });
  SB();
  sfor<0, 7>([&](auto I) { constexpr int j = 2 * decltype(I)::v + 1; kstep(IC<j>{}, mn[j], false); });
  put(IC<1>{});
  f32x4 keep1[2] = {acc[0][0], acc[0][1]};
  BAR();
  fin(IC<1>{}, keep1);
  TS(14);
}

extern "C" void kernel_launch(void* const* d_in, const int* in_sizes, int n_in, void* d_out, int out_size,
                              void* d_ws, size_t ws_size, hipStream_t stream) {
  Args a;
  a.x = (const char*)d_in[0]; a.wqkv = (const char*)d_in[1]; a.wo = (const char*)d_in[2];
  a.wg = (const char*)d_in[4]; a.wu = (const char*)d_in[5]; a.wd = (const char*)d_in[6];
  a.out = (char*)d_out; a.ws = (char*)d_ws;
  fused_block_kernel<<<256, 256, 0, stream>>>(a);
}
```

```cpp
#include <hip/hip_runtime.h>
#include <stdint.h>

typedef unsigned u32;
typedef unsigned long long u64;
typedef u32 u32x2 __attribute__((ext_vector_type(2)));
typedef u32 u32x4 __attribute__((ext_vector_type(4)));
typedef float f32x2 __attribute__((ext_vector_type(2)));
typedef float f32x4 __attribute__((ext_vector_type(4)));
typedef float f32x16 __attribute__((ext_vector_type(16)));
typedef short s16x4 __attribute__((ext_vector_type(4)));
typedef __bf16 bf16x2 __attribute__((ext_vector_type(2)));
typedef __bf16 bf16x8 __attribute__((ext_vector_type(8)));
#define LDS_AS __attribute__((address_space(3)))
#define GLB_AS __attribute__((address_space(1)))
#define DEVI __device__ __forceinline__

#define DBG_TS 0

template <int N> struct IC { static constexpr int v = N; };
template <int B, int E, class F> DEVI void sfor(F&& f) {
  if constexpr (B < E) { f(IC<B>{}); sfor<B + 1, E>(f); }
}

constexpr u32 WS_EPOCH = 0;
constexpr u32 WS_F1 = 4096;
constexpr u32 WS_F2 = 16384;
constexpr u32 WS_F3 = 393216;
constexpr u32 WS_HSTAT = 65536;
constexpr u32 WS_DBG = 524288;
constexpr u32 WS_OBUF = 1u << 20;
constexpr u32 WS_HBUF = 3u << 20;
constexpr u32 WS_MBUF = 5u << 20;
constexpr u32 WS_SIZE = 12u << 20;

constexpr u32 L_RING = 81920;
constexpr u32 L_SCR = 155648;
constexpr u32 L_TOP = 131072;
constexpr u32 L_HT = 86016;
constexpr u32 L_ST = 102400;
constexpr u32 L_DS = 110592;
constexpr u32 L_DX = 57344;

DEVI u32x4 lds_r128(u32 a) { return *(LDS_AS u32x4*)(size_t)(a); }
DEVI u32x2 lds_r64(u32 a) { return *(LDS_AS u32x2*)(size_t)(a); }
DEVI u32 lds_r32(u32 a) { return *(LDS_AS u32*)(size_t)(a); }
DEVI void lds_w128(u32 a, u32x4 v) { *(LDS_AS u32x4*)(size_t)(a) = v; }
DEVI void lds_w64(u32 a, u32x2 v) { *(LDS_AS u32x2*)(size_t)(a) = v; }
DEVI void lds_w32(u32 a, u32 v) { *(LDS_AS u32*)(size_t)(a) = v; }
DEVI u32x2 lds_tr(u32 a) {
  return __builtin_bit_cast(u32x2, __builtin_amdgcn_ds_read_tr16_b64_v4i16((LDS_AS s16x4*)(size_t)(a)));
}
DEVI u32x4 cat(u32x2 a, u32x2 b) { return u32x4{a.x, a.y, b.x, b.y}; }
DEVI f32x4 mma16(u32x4 a, u32x4 b, f32x4 c) {
  return __builtin_amdgcn_mfma_f32_16x16x32_bf16(__builtin_bit_cast(bf16x8, a), __builtin_bit_cast(bf16x8, b), c, 0, 0, 0);
}
DEVI f32x4 mma16k16(u32x2 a, u32x2 b, f32x4 c) {
  return __builtin_amdgcn_mfma_f32_16x16x16bf16_1k(__builtin_bit_cast(s16x4, a), __builtin_bit_cast(s16x4, b), c, 0, 0, 0);
}
DEVI f32x16 mma32(u32x4 a, u32x4 b, f32x16 c) {
  return __builtin_amdgcn_mfma_f32_32x32x16_bf16(__builtin_bit_cast(bf16x8, a), __builtin_bit_cast(bf16x8, b), c, 0, 0, 0);
}
DEVI u32 pk2(float a, float b) { bf16x2 v = {(__bf16)a, (__bf16)b}; return __builtin_bit_cast(u32, v); }
DEVI float bflo(u32 v) { return __uint_as_float(v << 16); }
DEVI float bfhi(u32 v) { return __uint_as_float(v & 0xffff0000u); }
DEVI float asf(u32 v) { return __uint_as_float(v); }
DEVI u32 asu(float v) { return __float_as_uint(v); }
#define SB() __builtin_amdgcn_sched_barrier(0)

DEVI void SETM0(u32 v) { asm volatile("s_mov_b32 m0, %0" ::"s"(v) : "memory"); }
DEVI void SETM0N(u32 v) { asm volatile("s_mov_b32 m0, %0\n\ts_nop 0" ::"s"(v) : "memory"); }
template <int OFF> DEVI void DMAn(u32 voff, u32x4 srd, u32 soff) {
  asm volatile("buffer_load_dwordx4 %0, %1, %2 offen offset:%3 lds" ::"v"(voff), "s"(srd), "s"(soff), "n"(OFF) : "memory");
}
template <int OFF> DEVI void DMAs(u32 voff, u32x4 srd, u32 soff) {
  asm volatile("buffer_load_dwordx4 %0, %1, %2 offen offset:%3 sc1 lds" ::"v"(voff), "s"(srd), "s"(soff), "n"(OFF) : "memory");
}
template <int OFF> DEVI void LDGv(u32x4& d, u32 voff, u32x4 srd, u32 soff) {
  asm volatile("buffer_load_dwordx4 %0, %1, %2, %3 offen offset:%4 sc1" : "=v"(d) : "v"(voff), "s"(srd), "s"(soff), "n"(OFF) : "memory");
}
template <int OFF> DEVI void LDGa(u32x4& d, u32 voff, u32x4 srd, u32 soff) {
  asm volatile("buffer_load_dwordx4 %0, %1, %2, %3 offen offset:%4 sc1" : "=a"(d) : "v"(voff), "s"(srd), "s"(soff), "n"(OFF) : "memory");
}
template <int OFF> DEVI void LDGr(u32x4& d, u32 voff, u32x4 srd, u32 soff) {
  asm volatile("buffer_load_dwordx4 %0, %1, %2, %3 offen offset:%4 sc1 ; reload" : "+v"(d) : "v"(voff), "s"(srd), "s"(soff), "n"(OFF) : "memory");
}
DEVI void LDGan(u32x4& d, u32 voff, u32x4 srd, u32 soff) {
  asm volatile("buffer_load_dwordx4 %0, %1, %2, %3 offen" : "=a"(d) : "v"(voff), "s"(srd), "s"(soff) : "memory");
}
template <int OFF> DEVI void LDG2n(u32x2& d, u32 voff, u32x4 srd, u32 soff) {
  asm volatile("buffer_load_dwordx2 %0, %1, %2, %3 offen offset:%4" : "=v"(d) : "v"(voff), "s"(srd), "s"(soff), "n"(OFF) : "memory");
}
DEVI void LDG2(u32x2& d, u32 voff, u32x4 srd, u32 soff) {
  asm volatile("buffer_load_dwordx2 %0, %1, %2, %3 offen sc1" : "=v"(d) : "v"(voff), "s"(srd), "s"(soff) : "memory");
}
DEVI void STG4(u32x4 d, u32 voff, u32x4 srd, u32 soff) {
  asm volatile("buffer_store_dwordx4 %0, %1, %2, %3 offen sc1" ::"v"(d), "v"(voff), "s"(srd), "s"(soff) : "memory");
}
DEVI void STG4a(const u32x4& d, u32 voff, u32x4 srd, u32 soff) {
  asm volatile("buffer_store_dwordx4 %0, %1, %2, %3 offen sc1" ::"a"(d), "v"(voff), "s"(srd), "s"(soff) : "memory");
}
DEVI void STF(u32x2 d, u32x4 srd, u32 soff) {
  asm volatile("s_mov_b64 exec, 1\n\tbuffer_store_dwordx2 %0, off, %1, %2 sc1\n\ts_mov_b64 exec, -1" ::"v"(d), "s"(srd), "s"(soff) : "memory");
}
template <int N, class T> DEVI void WAITv(T& x) { asm volatile("s_waitcnt vmcnt(%1) ; tie %0" : "+v"(x) : "n"(N) : "memory"); }
template <int N, class T> DEVI void WAITa(T& x) { asm volatile("s_waitcnt vmcnt(%1) ; tie %0" : "+a"(x) : "n"(N) : "memory"); }
template <class T> DEVI void TIE(T& x) { asm volatile("; tie %0" : "+v"(x) :: "memory"); }
template <class T> DEVI void TIEa(T& x) { asm volatile("; tie %0" : "+a"(x) :: "memory"); }
template <class T> DEVI void OPQa(T& v) { asm volatile("" : "+a"(v)); }
DEVI void TOUCH(u32& dummy, u32 voff, u32x4 srd, u32 soff) {
  asm volatile("buffer_load_dword %0, %1, %2, %3 offen" : "+v"(dummy) : "v"(voff), "s"(srd), "s"(soff) : "memory");
}
DEVI void MMA32_INIT(f32x16& acc, const u32x4& a, const u32x4& b) {
  asm volatile("v_mfma_f32_32x32x16_bf16 %0, %1, %2, 0" : "=&v"(acc) : "v"(a), "v"(b));
}
DEVI void MMA32_ACC(f32x16& acc, const u32x4& a, const u32x4& b) {
  asm volatile("v_mfma_f32_32x32x16_bf16 %0, %1, %2, %0" : "+v"(acc) : "v"(a), "v"(b));
}
DEVI void MMA32_ACC_A(f32x16& acc, const u32x4& a, const u32x4& b) {
  asm volatile("v_mfma_f32_32x32x16_bf16 %0, %1, %2, %0" : "+v"(acc) : "v"(a), "a"(b));
}
DEVI void MMA32W_INIT(f32x16& acc, const u32x4& a, const u32x4& b) {
  asm volatile("v_mfma_f32_32x32x16_bf16 %0, %1, %2, 0" : "=&v"(acc) : "a"(a), "v"(b));
}
DEVI void MMA32W_ACC(f32x16& acc, const u32x4& a, const u32x4& b) {
  asm volatile("v_mfma_f32_32x32x16_bf16 %0, %1, %2, %0" : "+v"(acc) : "a"(a), "v"(b));
}
DEVI void MMA16B_ACC(f32x4& acc, const u32x4& a, const u32x4& b) {
  asm volatile("v_mfma_f32_16x16x32_bf16 %0, %1, %2, %0" : "+v"(acc) : "v"(a), "a"(b));
}
DEVI void MMA16_INIT(f32x4& acc, const u32x4& a, const u32x4& b) {
  asm volatile("v_mfma_f32_16x16x32_bf16 %0, %1, %2, 0" : "=&v"(acc) : "v"(a), "v"(b));
}
DEVI void MMA16_ACC(f32x4& acc, const u32x4& a, const u32x4& b) {
  asm volatile("v_mfma_f32_16x16x32_bf16 %0, %1, %2, %0" : "+v"(acc) : "v"(a), "v"(b));
}
DEVI void MMA16K16(f32x4& d, const u32x2& a, const u32x2& b, const f32x4& c) {
  asm volatile("v_mfma_f32_16x16x16_bf16 %0, %1, %2, %3" : "=&v"(d) : "v"(a), "v"(b), "v"(c));
}
DEVI float vmax(float a, float b) { float r; asm("v_max_f32 %0, %1, %2" : "=v"(r) : "v"(a), "v"(b)); return r; }
DEVI float vmax3(float a, float b, float c) { float r; asm("v_max3_f32 %0, %1, %2, %3" : "=v"(r) : "v"(a), "v"(b), "v"(c)); return r; }
template <bool INIT> DEVI void MMA16(f32x4& acc, const u32x4& a, const u32x4& b) {
  if constexpr (INIT) MMA16_INIT(acc, a, b); else MMA16_ACC(acc, a, b);
}
DEVI float bcast_row0(float v) {
  auto a = __builtin_amdgcn_permlane32_swap(__float_as_uint(v), __float_as_uint(v), false, false);
  auto c = __builtin_amdgcn_permlane16_swap(a[0], a[0], false, false);
  return __uint_as_float(c[0]);
}
template <class T> DEVI void OPQ(T& v) { asm volatile("" : "+v"(v)); }
#define WAIT_VM(n) asm volatile("s_waitcnt vmcnt(%0)" ::"n"(n) : "memory")
#define WAIT_LGKM0() asm volatile("s_waitcnt lgkmcnt(0)" ::: "memory")
#define BAR() asm volatile("s_waitcnt lgkmcnt(0)\n\ts_barrier" ::: "memory")
#define BAR_VM(n) asm volatile("s_waitcnt vmcnt(%0) lgkmcnt(0)\n\ts_barrier" ::"n"(n) : "memory")

DEVI u32x4 mksrd(const void* p, u32 size) {
  u64 a = (u64)p;
  return u32x4{(u32)a, (u32)(a >> 32) & 0xffffu, size, 0x00020000u};
}
typedef __amdgpu_buffer_rsrc_t rsrc_t;
DEVI rsrc_t mkrs(const void* p, u32 size) {
  return __builtin_amdgcn_make_buffer_rsrc((void*)p, 0, size, 0x00020000);
}
constexpr int SC1 = 16;
constexpr u32 SPIN_MAX = 1u << 18;
#define SLP 3

struct Args {
  const char* x; const char* wqkv; const char* wo; const char* wg; const char* wu; const char* wd;
  char* out; char* ws;
};

DEVI u64 ld_flag(const char* ws, u32 off) {
  return __hip_atomic_load((GLB_AS u64*)(ws + off), __ATOMIC_RELAXED, __HIP_MEMORY_SCOPE_AGENT);
}
DEVI void st_flag(char* ws, u32 off, u64 v) {
  __hip_atomic_store((GLB_AS u64*)(ws + off), v, __ATOMIC_RELAXED, __HIP_MEMORY_SCOPE_AGENT);
}
DEVI void poll_flags(const char* ws, u32 off, int lane, int n, u64 tag) {
  u32 o = off + (u32)(lane < n ? lane : 0) * 8u;
  for (u32 it = 0; it < SPIN_MAX; ++it) {
    u64 f = ld_flag(ws, o);
    if (__all(f == tag)) break;
    __builtin_amdgcn_s_sleep(SLP);
  }
  asm volatile("" ::: "memory");
}

#define TS(i) do {} while (0)

#define K4 1
#define TLBW 0
#define KAW 1
#define HROT 1
#define TLB0 1
#define TJ1 0
#define TJ2 8
#define ST_CL -1
#define CLN 3
#define VQA 7
#define VQMAP {0, 1, 2, 3, 4, -1, 5, -1, 6, -1}
#define B2N 1
#define M3D 1
#define B4A 4
#define SCHED 0
#define ST_TD 9
#define TA_P2 1
#define ST_TA 4
#define ST_TG 4
#define ST_TO 4
constexpr u32 CODE_BYTES = 45056;

enum { K_NONE = 0, K_M0X, K_X, K_M0W, K_W, K_T, K_E, K_C, K_O };
struct MList {
  int n = 0; int k[48] = {}, a[48] = {}, b[48] = {};
  constexpr void add(int kk, int aa, int bb) { k[n] = kk; a[n] = aa; b[n] = bb; n++; }
  constexpr void X(int q) { add(K_M0X, q, 0); for (int m = 0; m < 4; m++) add(K_X, q, m); add(K_M0X, q, 4); add(K_X, q, 4); }
  constexpr void W(int st) { for (int m = 0; m < 2; m++) { add(K_M0W, st, m); for (int p = 0; p < 3; p++) add(K_W, st, m * 3 + p); } }
  constexpr void T(int s) { add(K_T, s, 0); }
};
constexpr MList stage_ops(int st) {
  MList L;
  if (st == -2) { L.X(0); L.W(0); L.add(K_E, 0, 0); }
  if (st == -1) { L.X(1); L.W(2); L.W(1); }
  if (st == 0) { L.W(3); }
  if (st == ST_TD) L.T(6);
  if (st == 1) { L.W(4); L.X(2); }
  if (st == 2) { L.W(5); }
  if (st == 3) { L.W(6); }
  if (st == 4) { L.X(3); L.W(7); }
  if (st >= ST_CL && st < ST_CL + 6 / CLN && ST_CL >= 0) for (int i = 0; i < CLN; i++) L.add(K_C, (st - ST_CL) * CLN + i, 0);
  if (st == ST_TO) L.T(7);
  if (st == ST_TA) L.T(5);

  return L;
}
constexpr bool is_vm(int k) { return k == K_X || k == K_W || k == K_T || k == K_E || k == K_C || k == K_O; }
constexpr int vm_allow(int st) {
  int cnt = 0;
  for (int s = -2; s < st; s++) {
    MList L = stage_ops(s);
    for (int i = 0; i < L.n; i++) {
      if (!is_vm(L.k[i])) continue;
      cnt++;
      if ((L.k[i] == K_W && L.a[i] == st) || (L.k[i] == K_X && L.a[i] == (st >> 1))) cnt = 0;
    }
  }
  return cnt;
}
constexpr u32 wreg(int st) { return st == 7 ? 0u : st == 3 ? 61440u : L_RING + (u32)((st > 3 ? st - 4 : st) * 24576); }
constexpr int gap_cap(int st, int g, int n) {
  int i = g % 18;
  if (st == 0 && g < 18) return i >= 4 ? 1 : 0;
  if (i >= 12) return 1;
  if (n > 12 && (i == 5 || i == 6 || i == 7 || i == 9 || i == 10 || i == 11)) return 1;
  return 0;
}
constexpr int gap_first(int st, int g, int n) { int c = 0; for (int i = 0; i < g; i++) c += gap_cap(st, i, n); return c; }

enum { E_NONE = 0, E_H, E_D, E_R, E_T, E_M, E_FL, E_FC, E_FS, E_ST, E_CL };
constexpr signed char P3K[256] = {1,0,1,0,1,0,1,0,1,0,1,0,1,0,1,0,4,0,0,0,2,0,2,0,2,0,2,0,1,0,1,0,1,0,1,0,2,0,2,0,2,0,2,0,1,0,1,0,1,0,1,0,2,0,2,0,2,0,2,0,1,0,1,0,9,0,1,0,1,0,1,0,1,0,1,0,1,0,0,0,0,0,0,0,0,0,0,0,0,0,2,0,2,0,2,0,2,0,9,0,1,0,1,0,1,0,1,0,8,0,2,0,2,0,2,0,2,0,1,0,1,0,1,0,1,0,2,0,2,0,2,0,2,0,9,0,1,0,1,0,1,0,8,0,1,0,1,0,1,0,1,0,1,0,0,0,1,0,1,0,9,0,1,0,1,0,6,0,1,0,1,0,1,0,1,0,2,0,2,0,2,0,2,0,2,0,2,0,2,0,2,0,9,0,0,0,5,0,5,0,5,0,5,0,5,0,5,0,5,0,5,0,5,0,5,0,5,0,5,0,5,0,5,0,9,0,2,0,2,0,2,0,2,0,2,0,2,0,2,0,2,0,2,0,2,0,2,0,2,0,5,0,5,0,0,9};
constexpr signed char P3A[256] = {2,0,2,0,2,0,2,0,2,0,2,0,2,0,2,0,0,0,0,0,3,0,3,0,3,0,3,0,3,0,3,0,3,0,3,0,4,0,4,0,4,0,4,0,3,0,3,0,3,0,3,0,5,0,5,0,5,0,5,0,4,0,4,0,0,0,4,0,4,0,4,0,4,0,4,0,4,0,0,0,0,0,0,0,0,0,0,0,0,0,6,0,6,0,6,0,6,0,1,0,5,0,5,0,5,0,5,0,0,0,7,0,7,0,7,0,7,0,5,0,5,0,5,0,5,0,8,0,8,0,8,0,8,0,2,0,6,0,6,0,6,0,1,0,6,0,6,0,6,0,6,0,6,0,0,0,7,0,7,0,3,0,7,0,7,0,0,0,7,0,7,0,7,0,7,0,9,0,9,0,9,0,9,0,10,0,10,0,10,0,10,0,4,0,0,0,0,0,1,0,2,0,3,0,4,0,5,0,6,0,7,0,8,0,9,0,10,0,11,0,12,0,13,0,5,0,11,0,11,0,11,0,11,0,12,0,12,0,12,0,12,0,13,0,13,0,13,0,13,0,14,0,15,0,0,6};
constexpr signed char P3B[256] = {0,0,1,0,2,0,3,0,4,0,5,0,6,0,7,0,0,0,0,0,0,0,1,0,2,0,3,0,0,0,1,0,2,0,3,0,0,0,1,0,2,0,3,0,4,0,5,0,6,0,7,0,0,0,1,0,2,0,3,0,0,0,1,0,0,0,2,0,3,0,4,0,5,0,6,0,7,0,0,0,0,0,0,0,0,0,0,0,0,0,0,0,1,0,2,0,3,0,0,0,0,0,1,0,2,0,3,0,0,0,0,0,1,0,2,0,3,0,4,0,5,0,6,0,7,0,0,0,1,0,2,0,3,0,0,0,0,0,1,0,2,0,0,0,3,0,4,0,5,0,6,0,7,0,0,0,0,0,1,0,0,0,2,0,3,0,0,0,4,0,5,0,6,0,7,0,0,0,1,0,2,0,3,0,0,0,1,0,2,0,3,0,0,0,0,0,0,0,0,0,0,0,0,0,0,0,0,0,0,0,0,0,0,0,0,0,0,0,0,0,0,0,0,0,0,0,0,0,1,0,2,0,3,0,0,0,1,0,2,0,3,0,0,0,1,0,2,0,3,0,0,0,0,0,0,0};
constexpr signed char P3RK[256] = {0,3,0,3,0,3,0,3,0,3,0,3,0,3,0,3,0,3,0,3,0,3,0,3,0,3,0,3,0,3,0,3,0,3,0,3,0,3,0,3,0,3,0,3,0,3,0,3,0,0,0,0,0,0,0,0,0,0,0,0,0,0,0,0,0,0,0,0,0,0,0,3,0,3,0,3,0,3,0,3,0,3,0,3,0,3,0,3,0,3,0,3,0,3,0,3,0,3,0,3,0,3,0,3,0,3,0,3,0,3,0,3,0,3,0,3,0,3,0,0,0,0,0,0,0,0,0,0,0,0,0,0,0,0,0,0,0,0,0,0,0,3,0,3,0,3,0,3,0,3,0,3,0,3,0,3,0,0,0,0,0,3,0,3,0,3,0,3,0,3,0,3,0,3,0,3,0,3,0,3,0,3,0,3,0,3,0,3,0,3,0,3,0,0,0,7,0,0,0,0,0,0,0,0,0,0,0,0,0,0,0,0,0,0,0,0,0,0,0,0,0,0,0,0,0,0,0,0,0,0,0,0,0,0,0,0,0,0,0,0,0,0,0,0,0,0,0,0,0,0,0,0,0,0,0,0};
constexpr signed char P3RA[256] = {0,0,0,0,0,0,0,0,0,0,0,0,0,0,0,0,0,1,0,1,0,1,0,1,0,1,0,1,0,1,0,1,0,2,0,2,0,2,0,2,0,2,0,2,0,2,0,2,0,0,0,0,0,0,0,0,0,0,0,0,0,0,0,0,0,0,0,0,0,0,0,3,0,3,0,3,0,3,0,3,0,3,0,3,0,3,0,4,0,4,0,4,0,4,0,4,0,4,0,4,0,4,0,5,0,5,0,5,0,5,0,5,0,5,0,5,0,5,0,0,0,0,0,0,0,0,0,0,0,0,0,0,0,0,0,0,0,0,0,0,0,6,0,6,0,6,0,6,0,6,0,6,0,6,0,6,0,0,0,0,0,7,0,7,0,7,0,7,0,7,0,7,0,7,0,7,0,8,0,8,0,8,0,8,0,8,0,8,0,8,0,8,0,0,0,0,0,0,0,0,0,0,0,0,0,0,0,0,0,0,0,0,0,0,0,0,0,0,0,0,0,0,0,0,0,0,0,0,0,0,0,0,0,0,0,0,0,0,0,0,0,0,0,0,0,0,0,0,0,0,0,0,0,0,0,0};
constexpr signed char P3RB[256] = {0,0,0,1,0,2,0,3,0,4,0,5,0,6,0,7,0,0,0,1,0,2,0,3,0,4,0,5,0,6,0,7,0,0,0,1,0,2,0,3,0,4,0,5,0,6,0,7,0,0,0,0,0,0,0,0,0,0,0,0,0,0,0,0,0,0,0,0,0,0,0,0,0,1,0,2,0,3,0,4,0,5,0,6,0,7,0,0,0,1,0,2,0,3,0,4,0,5,0,6,0,7,0,0,0,1,0,2,0,3,0,4,0,5,0,6,0,7,0,0,0,0,0,0,0,0,0,0,0,0,0,0,0,0,0,0,0,0,0,0,0,0,0,1,0,2,0,3,0,4,0,5,0,6,0,7,0,0,0,0,0,0,0,1,0,2,0,3,0,4,0,5,0,6,0,7,0,0,0,1,0,2,0,3,0,4,0,5,0,6,0,7,0,0,0,0,0,0,0,0,0,0,0,0,0,0,0,0,0,0,0,0,0,0,0,0,0,0,0,0,0,0,0,0,0,0,0,0,0,0,0,0,0,0,0,0,0,0,0,0,0,0,0,0,0,0,0,0,0,0,0,0,0,0,0,0};
constexpr int p3_cum(int G) { int c = 0; for (int g = 0; g < G; g++) c += P3K[g] != 0; return c; }
constexpr int p3_since(int Gi, int Gw) { return p3_cum(Gw) - p3_cum(Gi + 1); }
constexpr int p3_last(int k, int a) { int r = -1; for (int g = 0; g < 256; g++) if (P3K[g] == k && P3A[g] == a) r = g; return r; }
constexpr int p3_nm() { int c = 0; for (int g = 0; g < 256; g++) c += P3K[g] == E_M; return c; }
constexpr int P3_NM = p3_nm();
constexpr int p3_after(int k, int a, int G) { return p3_since(p3_last(k, a), G); }

constexpr int imin(int a, int b) { return a < b ? a : b; }
constexpr int P3_W0 = imin(imin(p3_after(E_D, 13, 256), p3_after(E_M, 13, 256)), imin(p3_after(E_D, 12, 256), p3_after(E_D, 11, 256)));
DEVI u32x4 selsrd(bool c, u32x4 a, u32x4 b) { return u32x4{c ? a.x : b.x, c ? a.y : b.y, c ? a.z : b.z, a.w}; }
struct Frag { u32x4 x[5]; u32x4 xs; u32x2 wq[2], wk[2], wv[2]; };

template <bool DO_P3>
DEVI void run_tail(const Args& A, const int lane, const int w, const int xcd, const int sl, const u64 tag,
                   u32x4 (&B2)[4][4], u32x2 (&xr)[2][2]
);

__global__ __launch_bounds__(256, 1) void fused_block_kernel(Args A) {
  __shared__ __attribute__((aligned(1024))) char smem[163840];
  asm volatile("" ::"v"((LDS_AS char*)smem) : "memory");
  const int tid = threadIdx.x, lane = tid & 63;
  const int w = __builtin_amdgcn_readfirstlane(tid >> 6);
  const int b = blockIdx.x, xcd = b & 7, sl = b >> 3;
  TS(0);
  const int t16 = lane & 15, q4 = lane >> 4, r8 = lane >> 3, c16 = lane & 7, qp = (lane & 15) >> 2;
  u32 tv = 0;
  {
    u64 pc;
    asm volatile("s_getpc_b64 %0" : "=s"(pc));
    u32x4 srd_c = u32x4{(u32)pc & ~127u, (u32)(pc >> 32) & 0xffffu, CODE_BYTES, 0x00020000u};
    TOUCH(tv, (u32)(((lane & 3) * 128 + sl * 4 + w) * 128), srd_c, 0u);
  }
  SB();
  TS(38);

  const int rg = 8 * (xcd >> 1) + (sl & 7), hq = 4 * (xcd & 1) + (sl >> 3);
  const int rt = 4 * xcd + (sl & 3), ct = sl >> 2;
  const bool p3 = sl < 28;
  const int fb = p3 ? 7 * (xcd & 3) + (sl >> 2) : 7 * (xcd & 3);
  const u32x4 srd_o = mksrd(A.wo, 512u * 1024u);
  const u32x4 srd_g = mksrd(A.wg, 512u * 3584u);
  const u32x4 srd_u = mksrd(A.wu, 512u * 3584u);
  const int row0 = 64 * rg - 16;
  u32 voff_x[5];
#pragma unroll
  for (int m = 0; m < 5; m++) {
    int gr = row0 + 16 * m + 4 * w;
    if (m == 0 && gr < 0) gr += 16;
    voff_x[m] = (u32)((gr + q4 + 4 - (m & 3) * (m < 4 ? 1 : 0)) * 1024) + (u32)((t16 ^ ((4 * w + q4) & 15)) * 16);
  }
  const u32 fW = ((r8 >> 1) & 1) | ((w & 1) << 1);
  u32 voff_w = (u32)(8 * w + r8) * 3072u + (u32)hq * 128u + (u32)((((c16 >> 1) ^ fW) * 2 + (c16 & 1)) * 16);
  u32 voff_w1 = voff_w + 32u * 3072u;
  const u32 rq = (u32)((((sl >> 1) ^ (sl >> 3)) & 1) * 2), rp = (u32)(sl & 1);
  u32 sW[8], sX[4];
  auto rot = [&](int i0, int i1) {
#pragma unroll
    for (int q = i0; q < i1; q++) {
      u32 pq = (q + rq) & 3u;
      sX[q] = pq * 256u;
      sW[2 * q] = (2u * pq + rp) * (64u * 3072u);
      sW[2 * q + 1] = (2u * pq + (rp ^ 1u)) * (64u * 3072u);
      asm volatile("" : "+s"(sX[q]), "+s"(sW[2 * q]), "+s"(sW[2 * q + 1]));
    }
  };
  rot(0, 1);
  {
    u32 m0a = (u32)w * 5120u, m0b = (u32)w * 3072u;
    asm volatile("" : "+v"(voff_x[0]), "+v"(voff_x[1]), "+v"(voff_x[2]), "+v"(voff_x[3]), "+v"(voff_x[4]), "+v"(voff_w), "+v"(voff_w1) : "s"(m0a), "s"(m0b));
    SB();
  }
  const u32x4 srd_x = mksrd(A.x - 4096, 2048u * 1024u + 4096u);
  const u32x4 srd_qkv = mksrd(A.wqkv, 512u * 3072u);
  u64 epoch = 0;
  u32 tlbw[3] = {0u, 0u, 0u};
  u32 vtg, voff_o, voff_r;
  u32x2 xr[2][2];
  u32x4 tb2[4][4];
  const int rg3 = 4 * (xcd >> 2) + (sl & 3);
  const u32x4 srd_cl = mksrd(A.ws, p3 ? WS_SIZE : 0u);
  u32x4 sent = {~0u, ~0u, ~0u, ~0u};
  auto mop = [&](auto ST, auto I, auto NOP) {
    constexpr int st = decltype(ST)::v, i = decltype(I)::v;
    constexpr bool nop = decltype(NOP)::v != 0;
    constexpr MList L = stage_ops(st);
    if constexpr (i < L.n) {
      constexpr int k = L.k[i], a = L.a[i], bb = L.b[i];
      auto m0 = [&](u32 v) { if constexpr (nop) SETM0N(v); else SETM0(v); };
      if constexpr (k == K_M0X) m0((u32)(a * 20480 + bb * 1024) + (u32)w * 5120u);
      if constexpr (k == K_X) DMAn<(bb & 3) * 1024>(voff_x[bb], srd_x, sX[a]);
      if constexpr (k == K_M0W) {
        if constexpr (a == 3 && bb == 1) m0(w < 2 ? 73728u + (u32)w * 3072u : L_SCR + 2048u + (u32)(w - 2) * 3072u);
        else m0(wreg(a) + (u32)(bb * 12288) + (u32)w * 3072u);
      }
      if constexpr (k == K_W) DMAn<(bb % 3) * 1024>((bb < 3 ? voff_w + 0u : voff_w1 + 0u), srd_qkv, sW[a]);
      if constexpr (k == K_C) STG4(sent, (u32)lane * 16u, srd_cl, WS_MBUF + (u32)((16 * rg3 + 4 * w + 1 + a / 2) * 56 + 2 * fb + (a & 1)) * 1024u);
      if constexpr (k == K_E) epoch = ld_flag(A.ws, WS_EPOCH);
      if constexpr (k == K_T) {
        if constexpr (a == 5) TOUCH(tv, vtg, selsrd(sl >= 16, srd_u, srd_g), 0u);
        if constexpr (a == 6) TOUCH(tv, (u32)lane * 128u, mksrd(A.wd, 1792u * 1024u), (u32)(448 * (sl >> 3) + ((sl & 7) * 4 + w) * 8) * 1024u);
        if constexpr (a == 7) TOUCH(tv, (u32)(lane & 31) * 128u, srd_o, (u32)(sl * 4 + w) * 4096u);
      }
    }
  };
  TS(39);
  u32 tl0[4];
  {
    u32 ox = (u32)(64 * rg + 16 * w) * 1024u, ow = sW[0] + (u32)w * 49152u, ow2 = sW[1] + (u32)w * 49152u;
    asm volatile("s_load_dword %0, %4, %6\n\ts_load_dword %1, %5, %7\n\ts_load_dword %2, %9, 0x0\n\ts_load_dword %3, %5, %8"
                 : "=&s"(tl0[0]), "=&s"(tl0[1]), "=&s"(tl0[2]), "=&s"(tl0[3]) : "s"(A.x), "s"(A.wqkv), "s"(ox), "s"(ow), "s"(ow2), "s"(A.ws) : "memory");
  }
  sfor<0, stage_ops(-2).n>([&](auto I) { mop(IC<-2>{}, I, IC<1>{}); });
  SB();
  TS(40);
  rot(1, 4);
  vtg = (u32)(((sl & 15) * 4 + w) * 8 + r8) * 3584u + (u32)(7 * (xcd & 3) + (c16 < 7 ? c16 : 0)) * 128u;
  OPQ(vtg);

  u32 xb[4], xb3[4], xsb[4];
#pragma unroll
  for (int a = 0; a < 4; a++) {
    xb[a] = (u32)(t16 >> 2) * 5120u + (u32)(t16 & 3) * 256u + (u32)((((((u32)a ^ (2u * rp)) << 2) | (u32)q4) ^ (u32)t16) << 4);
    xsb[a] = xb[a] + (u32)(w + 1) * 1024u;
    xb3[a] = xb[a] + 61440u;
    OPQ(xb[a]); OPQ(xsb[a]); OPQ(xb3[a]);
  }
  const u32 fR = ((lane >> 3) & 1) | (((lane >> 4) & 1) << 1);
  const u32 wlane = (u32)qp * 128u + (u32)(((u32)w ^ fR) * 32u) + (u32)(lane & 3) * 8u;
  u32 wbase = (u32)q4 * 3072u + wlane;
  u32 wbaseR = wbase + L_RING;
  u32 wbase7 = (q4 < 2 ? 73728u + (u32)q4 * 3072u : L_SCR + 2048u + (u32)(q4 - 2) * 3072u) + wlane;
  OPQ(wbase); OPQ(wbaseR); OPQ(wbase7);
  const u32 ONE2 = 0x3f803f80u;
  u32x4 ones = {ONE2, ONE2, ONE2, ONE2};
  OPQ(ones); OPQ(sent);

  f32x4 aq[4], ak[5], av[5], aqc, ag, as, ag0;
  const f32x4 z4 = {0.f, 0.f, 0.f, 0.f};

  auto rd1 = [&](auto KS, auto I, Frag& F) {
    constexpr int ks = decltype(KS)::v, i = decltype(I)::v;
    constexpr u32 xo = (ks >> 2) * 20480;
    constexpr u32 wa = ks == 7 ? 0u : wreg(ks >> 1) + (ks & 1) * 12288;
    constexpr u32 wo = wa >= L_RING ? wa - L_RING : wa;
    const u32 wb = ks == 7 ? wbase7 + 0u : wa >= L_RING ? wbaseR + 0u : wbase + 0u;
    const u32 xq = ks >= 12 ? xb3[ks & 3] + 0u : xb[ks & 3] + xo;
    if constexpr (i == 0) F.wq[0] = lds_tr(wb + wo);
    if constexpr (i == 1) F.wq[1] = lds_tr(wb + wo + 512);
    if constexpr (i >= 2 && i <= 5) F.x[i - 1] = lds_r128(xq + (i - 1) * 1024);
    if constexpr (i == 6) F.wk[0] = lds_tr(wb + wo + 1024);
    if constexpr (i == 7) F.wk[1] = lds_tr(wb + wo + 1024 + 512);
    if constexpr (i == 8) F.wv[0] = lds_tr(wb + wo + 2048);
    if constexpr (i == 9) F.wv[1] = lds_tr(wb + wo + 2048 + 512);
    if constexpr (i == 10) F.x[0] = lds_r128(xq);
    if constexpr (i == 11) F.xs = lds_r128(xsb[ks & 3] + xo);
  };
  auto mm1 = [&](auto I, Frag& F, auto FIRST) {
    constexpr int i = decltype(I)::v;
    constexpr bool in = decltype(FIRST)::v != 0;
    u32x4 wq = cat(F.wq[0], F.wq[1]), wk = cat(F.wk[0], F.wk[1]), wv = cat(F.wv[0], F.wv[1]);
    if constexpr (i < 4) MMA16<in>(aq[i], wq, F.x[i + 1]);
    else if constexpr (i < 8) MMA16<in>(ak[i - 3], wk, F.x[i - 3]);
    else if constexpr (i < 12) MMA16<in>(av[i - 7], F.x[i - 7], wv);
    else if constexpr (i == 12) MMA16<in>(aqc, wq, F.x[0]);
    else if constexpr (i == 13) MMA16<in>(ak[0], wk, F.x[0]);
    else if constexpr (i == 14) MMA16<in>(av[0], F.x[0], wv);
    else if constexpr (i == 15) MMA16<in>(ag0, F.x[0], F.x[0]);
    else if constexpr (i == 16) MMA16<in>(ag, F.xs, F.xs);
    else MMA16<in>(as, ones, F.xs);
  };
  u32 sta0 = q4 == qp ? L_SCR + (u32)t16 * 4u : L_SCR + 1024u + (u32)lane * 4u;
  u32 sta1 = q4 == qp ? L_SCR + (u32)(16 * (w + 1) + t16) * 4u : L_SCR + 1024u + (u32)lane * 4u;
  OPQ(sta0); OPQ(sta1);

  SB();
  sfor<0, stage_ops(-1).n>([&](auto I) { mop(IC<-1>{}, I, IC<1>{}); });
  const float NEG = -1e30f;
  f32x4 bC, bP, bP1;
#pragma unroll
  for (int i = 0; i < 4; i++) {
    int kap = 4 * q4 + i;
    bool par = ((t16 - kap) & 1) == 0;
    bC[i] = (par && kap <= t16) ? 0.f : NEG;
    bP[i] = (par && kap >= t16 + 2) ? 0.f : NEG;
    bP1[i] = rg == 0 ? NEG : bP[i];
  }
  const u32 half = (t16 & 1) ? 0xffff0000u : 0x0000ffffu;
  u32x4 mk;
#pragma unroll
  for (int a = 0; a < 2; a++) {
    bool isc = (4 * q4 + 2 * a + (t16 & 1)) <= t16;
    mk[2 + a] = isc ? half : 0u; mk[a] = isc ? 0u : half;
  }
  u32 stk = L_SCR + (u32)t16 * 4u, stv = L_SCR + (u32)q4 * 16u;
  u32 voff_g[4];
#pragma unroll
  for (int i = 0; i < 4; i++) {
    voff_g[i] = (u32)(8 * i + r8) * 3584u - (u32)i * 1024u + (u32)fb * 128u + (u32)((c16 ^ (4 * ((r8 >> 1) & 1))) * 16);
    OPQ(voff_g[i]);
  }
  voff_o = (u32)r8 * 1024u + (u32)c16 * 16u;
  voff_r = (u32)t16 * 1024u + (u32)q4 * 8u;
  OPQ(voff_r);
  OPQ(bC); OPQ(bP); OPQ(bP1); OPQ(mk); OPQ(stk); OPQ(stv); OPQ(voff_o);
  SB();
  TS(37);
  Frag FA, FB;
  sfor<0, 8>([&](auto ST) {
    constexpr int st = decltype(ST)::v;
    constexpr MList L = stage_ops(st);
    static_assert(gap_first(st, 36, L.n) >= L.n, "not enough slots");
    WAIT_VM(vm_allow(st));
    if constexpr ((st & 1) == 0) lds_w128((u32)((st >> 1) * 20480) + (u32)t16 * 16u, ones);
    BAR();
    if constexpr (st == 0) asm volatile("" ::"s"(tlbw[0]), "s"(tlbw[1]), "s"(tlbw[2]));
    if constexpr (st == 0) asm volatile("" ::"s"(tl0[0]), "s"(tl0[1]), "s"(tl0[2]), "s"(tl0[3]));
    if constexpr (st == 0) TS(1);
    if constexpr (st > 0) TS(16 + st);
    auto vm = [&](auto G) {
      constexpr int g = decltype(G)::v, f = gap_first(st, g, L.n), c = gap_cap(st, g, L.n);
      constexpr bool bare = st == 0 && g < 18;
      if constexpr (c >= 1) mop(ST, IC<f>{}, IC<(c == 2 || bare) ? 1 : 0>{});
      if constexpr (c >= 2) mop(ST, IC<f + 1>{}, IC<0>{});
    };
    sfor<0, 18>([&](auto I) {
      constexpr int i = decltype(I)::v;
      if constexpr (st > 0) mm1(I, FB, IC<0>{});
      SB();
      if constexpr (i < 12) rd1(IC<2 * st>{}, I, FA);
      SB();
      vm(I);
      SB();
    });
    sfor<0, 18>([&](auto I) {
      constexpr int i = decltype(I)::v;
      mm1(I, FA, IC<st == 0 ? 1 : 0>{});
      SB();
      if constexpr (i < 12) rd1(IC<2 * st + 1>{}, I, FB);
      SB();
      vm(IC<18 + i>{});
      SB();
    });
  });
  {
    const bool l1 = (lane & 1) != 0, l2 = (lane & 2) != 0;
    float sa, sb, s0, ma, mb;
    sfor<0, 18>([&](auto I) {
      constexpr int i = decltype(I)::v;
      mm1(IC<17 - i>{}, FB, IC<0>{});
      SB();
      if constexpr (i == 9) { sa = l1 ? ag[1] : ag[0]; sb = l1 ? ag[3] : ag[2]; }
      if constexpr (i == 10) { sa = l2 ? sb : sa; ma = as[0] * (1.f / 512.f); }
      if constexpr (i == 11) { sb = l1 ? ag0[1] : ag0[0]; s0 = l1 ? ag0[3] : ag0[2]; }
      if constexpr (i == 12) { sb = l2 ? s0 : sb; sa *= (1.f / 512.f); }
      if constexpr (i == 13) { s0 = bcast_row0(ag0[0]); }
      if constexpr (i == 14) { sa = fmaxf(__builtin_fmaf(-ma, ma, sa), 0.f); }
      if constexpr (i == 15) { mb = s0 * (1.f / 512.f); sb *= (1.f / 512.f); }
      if constexpr (i == 16) { sa = __builtin_amdgcn_rsqf(sa + 1e-5f); }
      if constexpr (i == 17) { sb = fmaxf(__builtin_fmaf(-mb, mb, sb), 0.f); }
      if constexpr (i == 9) OPQ(as);
      if constexpr (i >= 9) { OPQ(sa); OPQ(sb); }
      if constexpr (i >= 10) OPQ(ma);
      if constexpr (i >= 11 && i < 15) OPQ(s0);
      if constexpr (i >= 15) OPQ(mb);
      SB();
    });
    TS(2);
    lds_w32(sta1, asu(sa)); lds_w32(sta1 + 512u, asu(-ma * sa));
    sb = __builtin_amdgcn_rsqf(sb + 1e-5f);
    lds_w32(sta0, asu(sb)); lds_w32(sta0 + 512u, asu(-mb * sb));
  }

  BAR();
  TS(41);
  auto vg = [&](auto N) {
    constexpr int ord[12] = {0, 1, 4, 2, 5, 3, 6, 7, 8, 9, 10, 11};
    constexpr int g = ord[decltype(N)::v], sg = g - 4;
    SB();
    if constexpr (g < 4) {
      sfor<0, 4>([&](auto I) { LDGan(tb2[g][decltype(I)::v], voff_o, srd_o, (u32)((128 * w + 32 * g + 8 * decltype(I)::v) * 1024 + ct * 128)); });
    } else {
      if constexpr (sg == 4) {
#pragma unroll
        for (int i = 0; i < 4; i++) { voff_g[i] ^= 32u; OPQ(voff_g[i]); }
      }
      SETM0N((u32)(sg * 4 + w) * 4096u);
      sfor<0, 4>([&](auto I) {
        constexpr int i = decltype(I)::v;
        DMAn<i * 1024>(voff_g[i], sg >= 4 ? srd_u + 0u : srd_g + 0u, (u32)((sg & 3) * 4 + w) * (32u * 3584u));
      });
    }
    SB();
  };
  auto vq = [&](auto S) {
    constexpr int map[10] = VQMAP;
    if constexpr (map[decltype(S)::v] >= 0) vg(IC<map[decltype(S)::v]>{});
  };
  float rs[5], nm[5];
  f32x4 rs4[5], nm4[5];
#pragma unroll
  for (int T = 0; T < 5; T++) { rs[T] = asf(lds_r32(stk + T * 64)); nm[T] = asf(lds_r32(stk + 512 + T * 64)); }
#pragma unroll
  for (int T = 0; T < 5; T++) {
    rs4[T] = __builtin_bit_cast(f32x4, lds_r128(stv + T * 64));
    nm4[T] = __builtin_bit_cast(f32x4, lds_r128(stv + 512 + T * 64));
  }
  SB();
  f32x4 csq, csk;
#pragma unroll
  for (int i = 0; i < 4; i++) {
    csq[i] = asf((u32)__builtin_amdgcn_mov_dpp((int)asu(aqc[i]), 0x150, 0xf, 0xf, false));
    csk[i] = asf((u32)__builtin_amdgcn_mov_dpp((int)asu(ak[0][i]), 0x150, 0xf, 0xf, false));
  }
  float csv = bcast_row0(av[0][0]);
  vq(IC<0>{});
  const float QS = 0.25f * 1.4426950408889634f;
  u32x2 qf[4], kf[5], vf[5];
  f32x4 sp[4], sc[4];
  sfor<0, 5>([&](auto TT) {
    constexpr int T = decltype(TT)::v;
    f32x4 kk = ak[T] * rs[T] + csk * nm[T];
    kf[T] = u32x2{pk2(kk[0], kk[1]), pk2(kk[2], kk[3])};
    if constexpr (T > 0) {
      f32x4 qq = aq[T - 1] * rs[T] + csq * nm[T];
      qf[T - 1] = u32x2{pk2(qq[0], qq[1]), pk2(qq[2], qq[3])};
    }
    SB();
    if constexpr (T > 1) {
      MMA16K16(sp[T - 2], kf[T - 2], qf[T - 2], T == 2 ? bP1 : bP);
      MMA16K16(sc[T - 2], kf[T - 1], qf[T - 2], bC);
    }
    if constexpr (T == 1) vq(IC<1>{});
    if constexpr (T == 3) vq(IC<2>{});
  });
  vq(IC<3>{});
  asm volatile("s_nop 1");
  MMA16K16(sp[3], kf[3], qf[3], bP);
  MMA16K16(sc[3], kf[4], qf[3], bC);
  SB();
  sfor<0, 5>([&](auto TT) {
    constexpr int T = decltype(TT)::v;
    f32x4 v = av[T] * rs4[T] + nm4[T] * csv;
    vf[T] = u32x2{pk2(v[0], v[1]), pk2(v[2], v[3])};
    if constexpr (T == 1) vq(IC<4>{});
  });
  vq(IC<5>{});
  float e0[4], e1[4], mx[4];
#pragma unroll
  for (int j = 0; j < 4; j++) {
    e0[j] = vmax(vmax3(sp[j][0], sp[j][1], sc[j][0]), sc[j][1]);
    e1[j] = vmax(vmax3(sp[j][2], sp[j][3], sc[j][2]), sc[j][3]);
    mx[j] = vmax(e0[j], e1[j]);
  }
  vq(IC<6>{});
  {
    auto s01 = __builtin_amdgcn_permlane16_swap(asu(mx[0]), asu(mx[1]), false, false);
    auto s23 = __builtin_amdgcn_permlane16_swap(asu(mx[2]), asu(mx[3]), false, false);
    float ab = vmax(asf(s01[0]), asf(s01[1]));
    float cd = vmax(asf(s23[0]), asf(s23[1]));
    auto s2 = __builtin_amdgcn_permlane32_swap(asu(ab), asu(cd), false, false);
    float R = vmax(asf(s2[0]), asf(s2[1])) * QS;
    auto r2 = __builtin_amdgcn_permlane32_swap(asu(R), asu(R), false, false);
    auto ra = __builtin_amdgcn_permlane16_swap(r2[0], r2[0], false, false);
    auto rc = __builtin_amdgcn_permlane16_swap(r2[1], r2[1], false, false);
    mx[0] = asf(ra[0]); mx[1] = asf(ra[1]); mx[2] = asf(rc[0]); mx[3] = asf(rc[1]);
  }
  vq(IC<7>{});
  u32x2 ob[4];
  u32x4 P[4], vv[4];
  f32x4 o[4], sm[4];
  sfor<0, 4>([&](auto JJ) {
    constexpr int j = decltype(JJ)::v;
    float p0 = __builtin_amdgcn_exp2f(__builtin_fmaf(e0[j], QS, -mx[j]));
    float p1 = __builtin_amdgcn_exp2f(__builtin_fmaf(e1[j], QS, -mx[j]));
    u32 k0 = pk2(p0, p0), k1 = pk2(p1, p1);
    P[j] = u32x4{k0 & mk[0], k1 & mk[1], k0 & mk[2], k1 & mk[3]};
    vv[j] = cat(vf[j], vf[j + 1]);
    OPQ(P[j]); OPQ(vv[j]);
    SB();
    if constexpr (j > 0) { MMA16_INIT(sm[j - 1], ones, P[j - 1]); MMA16_INIT(o[j - 1], vv[j - 1], P[j - 1]); }
  });
  asm volatile("s_nop 1");
  MMA16_INIT(sm[3], ones, P[3]); MMA16_INIT(o[3], vv[3], P[3]);
  vq(IC<8>{});
  SB();
#pragma unroll
  for (int j = 0; j < 4; j++) {
    float inv = __builtin_amdgcn_rcpf(sm[j][0]);
    f32x4 on = o[j] * inv;
    ob[j] = u32x2{pk2(on[0], on[1]), pk2(on[2], on[3])};
    asm volatile("" ::"v"(sm[j]));
    SB();
  }
  const rsrc_t rs_ws = mkrs(A.ws, WS_SIZE);
  const int head = 4 * hq + w;
  const u32 oo = WS_OBUF + (u32)((2 * rg) * 32 + head) * 1024u;
  __builtin_amdgcn_raw_buffer_store_b128(cat(ob[0], ob[2]), rs_ws, lane * 16, oo, SC1);
  __builtin_amdgcn_raw_buffer_store_b128(cat(ob[1], ob[3]), rs_ws, lane * 16, oo + 32768u, SC1);
  SB();
  TS(3);
  vg(IC<VQA>{}); vg(IC<VQA + 1>{});
  u32x4 B2[4][4];
  WAIT_VM(4 * (VQA - 6) + 10);
  sfor<0, 16>([&](auto I) { TIEa(tb2[decltype(I)::v >> 2][decltype(I)::v & 3]); });
  auto b2t = [&](auto KS) {
    constexpr int ks = decltype(KS)::v;
    const u32 f2 = ((q4 & 1) << 1) | (qp >> 1), rb = L_TOP + (u32)w * 4096u;
    const u32 wr_d = rb + (u32)r8 * 128u + (u32)((((c16 >> 1) ^ ((r8 >> 1) & 3)) * 2 + (c16 & 1)) * 16);
#pragma unroll
    for (int o = 0; o < 4; o++) lds_w128(wr_d + o * 1024, tb2[ks][o]);
    SB();
#pragma unroll
    for (int c = 0; c < 4; c++) {
      u32 a = rb + (u32)(4 * q4 + qp) * 128u + (u32)(((u32)(c ^ (2 * (w & 1))) ^ f2) * 32u) + (u32)(lane & 3) * 8u;
      B2[ks][c] = cat(lds_tr(a), lds_tr(a + 2048));
    }
    SB();
  };
  sfor<0, B2N>([&](auto KS) { b2t(KS); });
  WAIT_VM(8);
  asm volatile("" ::"v"(tv));
  const u64 tag = epoch + 0x9E3779B97F4A7C15ull;
  if (lane == 0) st_flag(A.ws, WS_F1 + (u32)(rg * 32 + head) * 8u, tag);
  TS(4);
  sfor<0, 4>([&](auto I) {
    constexpr int s = decltype(I)::v >> 1, c = decltype(I)::v & 1;
    LDG2n<c * 32>(xr[s][c], voff_r, srd_x, (u32)((64 * rt + 32 * s + 16 * (w >> 1) + 4) * 1024 + ct * 128 + (w & 1) * 64));
  });
  sfor<VQA + 2, 12>([&](auto N) { vg(N); });
  sfor<B2N, 4>([&](auto KS) { b2t(KS); });
  if (!p3) { run_tail<false>(A, lane, w, xcd, sl, tag, B2, xr); __builtin_amdgcn_endpgm(); }
  run_tail<true>(A, lane, w, xcd, sl, tag, B2, xr);
}

template <bool DO_P3>
DEVI void run_tail(const Args& A, const int lane, const int w, const int xcd, const int sl, const u64 tag,
                   u32x4 (&B2)[4][4], u32x2 (&xr)[2][2]
) {
  const int t16 = lane & 15, q4 = lane >> 4, r8 = lane >> 3, c16 = lane & 7, qp = (lane & 15) >> 2;
  const int rt = 4 * xcd + (sl & 3), ct = sl >> 2;
  const rsrc_t rs_ws = mkrs(A.ws, WS_SIZE);
  const rsrc_t rs_x = mkrs(A.x, 2048u * 1024u);
  const rsrc_t rs_d = mkrs(A.wd, 1792u * 1024u);
  const rsrc_t rs_out = mkrs(A.out, 2048u * 1024u);
  const f32x4 z4 = {0.f, 0.f, 0.f, 0.f};
  const int utt = w >> 1, uG = w & 1;
  const u32 ONE2 = 0x3f803f80u;

  const int ctd = ct;
  const u32x4 srd_ws = mksrd(A.ws, WS_SIZE);
  const u32x4 srd_d = mksrd(A.wd, 1792u * 1024u);
  const u32x2 tag2 = {(u32)tag, (u32)(tag >> 32)};
  const u32 f2 = ((q4 & 1) << 1) | (qp >> 1);
  const u32 ring = L_TOP + (u32)w * 8192u;
  const u32 chs = (u32)((((c16 >> 1) ^ ((r8 >> 1) & 3)) * 2 + (c16 & 1)) * 16);

  u32 tv = 0;
  auto touchD = [&](auto J0, auto J1) {
    constexpr int j0 = decltype(J0)::v, n = decltype(J1)::v - j0;
    TOUCH(tv, (u32)(lane < n * 8 ? lane : 0) * 128u, srd_d, (u32)(448 * (sl >> 3) + 32 * j0 + ((sl & 7) * 4 + w) * n) * 1024u);
  };
  auto touchA = [&]() { touchD(IC<TJ1>{}, IC<TJ2>{}); };
  SB();
  if constexpr (TJ1 > 0) touchD(IC<0>{}, IC<TJ1>{});
  TS(44);
  BAR_VM(TJ1 > 0 ? 1 : 0);
  TIE(xr[0][0]); TIE(xr[0][1]); TIE(xr[1][0]); TIE(xr[1][1]);
  TS(7);
  u32x4 Wf[32];
  u32 wgb = (u32)(q4 & 1) * 65536u + (u32)(4 * (q4 >> 1) + qp) * 128u + (((u32)w * 32u) ^ (u32)((qp >> 1) * 64) ^ (u32)((q4 & 1) * 32)) + (u32)(lane & 3) * 8u;
  OPQ(wgb);
  auto wg_rd = [&](auto K) {
    constexpr int k = decltype(K)::v;
    if constexpr (DO_P3) {
      Wf[k] = cat(lds_tr(wgb + (k >> 1) * 4096 + (k & 1) * 1024), lds_tr(wgb + (k >> 1) * 4096 + (k & 1) * 1024 + 2048));
    }
  };
  f32x4 csa = z4;

  u32x4 hres[2];
  {
    u64 fl = ld_flag(A.ws, WS_F1 + (u32)(rt * 32 + 8 * w + (lane & 7)) * 8u);
    SB();
    sfor<0, 16>([&](auto K) { wg_rd(K); });
    SB();
    if (!__all(fl == tag)) poll_flags(A.ws, WS_F1 + (u32)(rt * 32 + 8 * w) * 8u, lane, 8, tag);
    TS(5);
    u32x4 ol[2][8];
    sfor<0, 16>([&](auto I) {
      constexpr int t = 1 - (decltype(I)::v >> 3), i = decltype(I)::v & 7;
      LDGv<(i & 3) * 1024>(ol[t][i], (u32)lane * 16u, srd_ws, WS_OBUF + (u32)((2 * rt + (t ^ utt)) * 32 + 8 * w + (i & 4)) * 1024u);
    });
    SB();
    sfor<16, 32>([&](auto K) { wg_rd(K); });
    SB();
    if constexpr (TA_P2 == 3) touchA();
    const u32 own = ring + (u32)lane * 16u;
    const u32 pa = L_TOP + (u32)(w ^ 2) * 8192u + (u32)lane * 16u;
    const u32 pb = L_TOP + (u32)(w ^ 1) * 8192u + (u32)lane * 16u;
    f32x4 acc[2][2][4];
    auto gemm = [&](auto T) {
      constexpr int t = decltype(T)::v;
      sfor<0, 4>([&](auto KS) {
        constexpr int ks = decltype(KS)::v;
        constexpr int left = 14 - 8 * (1 - t) - 2 * ks + (TA_P2 >= 2 || (t == 0 && ks >= 2) ? 1 : 0);
        if constexpr (TA_P2 == 1 && t == 0 && ks == 2) touchA();
        WAITv<left>(ol[t][2 * ks]); TIE(ol[t][2 * ks + 1]);
        u32x4 La = ol[t][2 * ks], Lb = ol[t][2 * ks + 1];
        u32x4 b0 = {La.x, La.y, Lb.x, Lb.y}, b1 = {La.z, La.w, Lb.z, Lb.w};
#pragma unroll
        for (int c = 0; c < 4; c++) {
          acc[t][0][c] = mma16(B2[ks][c], b0, ks == 0 ? z4 : acc[t][0][c]);
          acc[t][1][c] = mma16(B2[ks][c], b1, ks == 0 ? z4 : acc[t][1][c]);
          if constexpr (t == 0) {
            if (c & 1) lds_w128(pa + (ks * 2 + (c >> 1)) * 1024, __builtin_bit_cast(u32x4, acc[1][ks >> 1][(ks & 1) * 2 + (c >> 1)]));
          }
        }
        if constexpr (t == 0) {
#pragma unroll
          for (int i = 0; i < 2; i++) {
            __builtin_amdgcn_sched_group_barrier(0x008, 4, 0);
            __builtin_amdgcn_sched_group_barrier(0x200, 1, 0);
          }
        }
        SB();
      });
    };
    gemm(IC<1>{});
    SB();
    gemm(IC<0>{});
    TS(42);
    BAR();
#pragma unroll
    for (int c = 3; c >= 0; c--)
#pragma unroll
      for (int s = 0; s < 2; s++) acc[0][s][c] += __builtin_bit_cast(f32x4, lds_r128(own + (s * 4 + c) * 1024));
    WAIT_LGKM0();
#pragma unroll
    for (int s = 0; s < 2; s++)
#pragma unroll
      for (int c = 0; c < 2; c++) lds_w128(own + (s * 2 + c) * 1024, __builtin_bit_cast(u32x4, acc[0][s][2 + c]));
#pragma unroll
    for (int s = 0; s < 2; s++)
#pragma unroll
      for (int c = 0; c < 2; c++)
        acc[0][s][c] += f32x4{bflo(xr[s][c].x), bfhi(xr[s][c].x), bflo(xr[s][c].y), bfhi(xr[s][c].y)};
    BAR();
    float y[2];
#pragma unroll
    for (int s = 0; s < 2; s++) {
      f32x4 r0 = acc[0][s][0] + __builtin_bit_cast(f32x4, lds_r128(pb + (s * 2) * 1024));
      f32x4 r1 = acc[0][s][1] + __builtin_bit_cast(f32x4, lds_r128(pb + (s * 2 + 1) * 1024));
      u32x4 h = {pk2(r0[0], r0[1]), pk2(r0[2], r0[3]), pk2(r1[0], r1[1]), pk2(r1[2], r1[3])};
      __builtin_amdgcn_raw_buffer_store_b128(h, rs_ws, (u32)(q4 >> 1) * 1024u + (u32)((q4 & 1) * 32 + t16) * 16u,
                                             WS_HBUF + (u32)((xcd * 8 + 4 * s + 2 * utt + ((sl >> 1) & 1)) * 32 + 2 * (2 * ct + uG)) * 1024u + (u32)(sl & 1) * 256u, SC1);
      float sm = 0.f, sq = 0.f;
#pragma unroll
      for (int i = 0; i < 4; i++) {
        float a = bflo(h[i]), c = bfhi(h[i]);
        sm += a + c; sq += a * a + c * c;
      }
      hres[s] = h;
      OPQ(hres[s]);
      auto x1 = __builtin_amdgcn_permlane16_swap(asu(sm), asu(sq), false, false);
      y[s] = asf(x1[0]) + asf(x1[1]);
    }
    auto x2 = __builtin_amdgcn_permlane32_swap(asu(y[0]), asu(y[1]), false, false);
    float zz = asf(x2[0]) + asf(x2[1]);
    __builtin_amdgcn_raw_buffer_store_b32(asu(zz), rs_ws, (u32)t16 * 16u + (u32)q4 * 4u,
                                          WS_HSTAT + (u32)((rt * 16 + 2 * ct + uG) * 32 + 16 * utt) * 16u, SC1);
    SB();
    TS(43);
  }
  u32 voff_d[4];
#pragma unroll
  for (int i = 0; i < 4; i++) { voff_d[i] = (u32)r8 * 1024u + chs + (u32)i * 7168u; OPQ(voff_d[i]); }
  const u32 dsb = L_DS + (u32)w * 12288u, dsx = L_DX + (u32)w * 8192u;
  const u32 dso = (u32)(448 * w) * 1024u + (u32)ctd * 128u;
  auto d_m0 = [&](auto J, auto NOP) {
    constexpr int j = decltype(J)::v;
    u32 v = j >= 12 ? dsx + (u32)(j - 12) * 4096u : dsb + (u32)(j % 3) * 4096u;
    if constexpr (decltype(NOP)::v != 0) SETM0N(v); else SETM0(v);
  };
  auto d_dma = [&](auto J, auto I) {
    constexpr int j = decltype(J)::v, i = decltype(I)::v;
    DMAn<i * 1024>(voff_d[i], srd_d, dso + (u32)j * 32768u);
  };
  auto d_all = [&](auto J) { d_m0(J, IC<1>{}); sfor<0, 4>([&](auto I) { d_dma(J, I); }); };
  BAR_VM(0);
  asm volatile("" ::"v"(tv));
  if (w == 0) STF(tag2, srd_ws, WS_F2 + (u32)(rt * 8 + ct) * 8u);
  sfor<0, 3>([&](auto J) { d_all(J); });
  if constexpr (DO_P3) {
      u32 v = ((t16 == 0 && !(q4 & 1)) || (t16 == 4 && (q4 & 1))) ? ONE2 : 0u;
      u32x4 sel = {v, v, v, v};
      OPQ(sel); OPQ(csa);
      asm volatile("s_nop 1");
      sfor<0, 32>([&](auto K) { MMA16B_ACC(csa, sel, Wf[decltype(K)::v]); });
      asm volatile("s_nop 7");
    }
  TS(6);

  u32 b4c[4];
#pragma unroll
  for (int c = 0; c < 4; c++) {
    b4c[c] = dsb + (u32)(16 * (q4 >> 1) + 4 * (q4 & 1) + qp) * 128u + (u32)(((u32)c ^ f2) * 32u) + (u32)(lane & 3) * 8u;
    OPQ(b4c[c]);
  }
  u32x2 B4h;
  u32x4 B4[14][4];
  const u32 dxd = dsx - dsb;
  auto d_rd = [&](auto J, auto O) {
    constexpr int j = decltype(J)::v, o = decltype(O)::v;
    u32x2 v = lds_tr(b4c[o >> 1] + (j >= 12 ? dxd + (j - 12) * 4096 : (j % 3) * 4096) + (o & 1) * 1024);
    if constexpr (o & 1) B4[j][o >> 1] = cat(B4h, v); else B4h = v;
  };
  auto d_rd8 = [&](auto J) { sfor<0, 8>([&](auto O) { d_rd(J, O); }); };
  const u32 l16 = (u32)lane * 16u;
  u32x4 mf[4][14];
  const u32 mso = WS_MBUF + (u32)(4 * rt * 56 + 14 * w) * 1024u;
  auto m_load = [&](auto Q, auto J) {
    constexpr int q = decltype(Q)::v, j = decltype(J)::v;
    if constexpr (DO_P3 && (q == 1 || q == 2)) LDGa<(j & 3) * 1024>(mf[q][j], l16, srd_ws, mso + (u32)(q * 56 + (j & ~3)) * 1024u);
    else LDGv<(j & 3) * 1024>(mf[q][j], l16, srd_ws, mso + (u32)(q * 56 + (j & ~3)) * 1024u);
  };
  const u32 f3p = WS_F3 + (u32)((xcd * 2 + ((sl >> 1) & 1)) * 32 + 7 * w) * 32u;
  u32 f3v = (u32)(lane < 28 ? lane : 0) * 8u;
  OPQ(f3v);
  u32x2 fq;
  auto f_ok = [&]() { return __all(fq.x == tag2.x && fq.y == tag2.y) != 0; };
  auto f_spin = [&](u32 off, auto SL) {
    for (u32 it = 0; it < SPIN_MAX; ++it) {
      if (f_ok()) break;
      __builtin_amdgcn_s_sleep(decltype(SL)::v);
      LDG2(fq, f3v, srd_ws, off);
      WAITv<0>(fq);
    }
  };

  f32x4 pc[2][4];
  auto mm4 = [&](auto Q, auto I) {
    constexpr int q = decltype(Q)::v, j = decltype(I)::v >> 2, c = decltype(I)::v & 3;
    pc[q & 1][c] = mma16(B4[j][c], mf[q][j], j == 0 ? z4 : pc[q & 1][c]);
  };
  auto kstep = [&](auto Q, auto J) { sfor<0, 4>([&](auto C) { mm4(Q, IC<4 * decltype(J)::v + decltype(C)::v>{}); }); };
  auto mtie = [&](auto Q) {
    constexpr int q = decltype(Q)::v;
    sfor<0, 14>([&](auto J) { if constexpr (DO_P3 && (q == 1 || q == 2)) TIEa(mf[q][decltype(J)::v]); else TIE(mf[q][decltype(J)::v]); });
  };
  constexpr int N1 = DO_P3 ? 28 - P3_NM : 14, NV = DO_P3 ? imin(N1 + 14, 19) : 14;
  constexpr int R0 = DO_P3 ? 16 : 0, SP = 18;
  auto piece0 = [&](auto&& side) {
    mtie(IC<0>{});
    sfor<0, 56>([&](auto I) {
      constexpr int i = decltype(I)::v, n = i / 3, r = i - R0;
      mm4(IC<0>{}, I);
      SB();
      if constexpr (r >= 0 && r < 20) {
        if constexpr (DO_P3 && (r & 3) == 0) WAIT_VM(p3_after(E_D, 9 + r / 4, 256) + (i + 1) / 3 + (i > SP ? 1 : 0));
        d_rd(IC<9 + r / 4>{}, IC<2 * (r & 3)>{}); d_rd(IC<9 + r / 4>{}, IC<2 * (r & 3) + 1>{});
      }
      if constexpr (i % 3 == 1 && n < NV) {
        if constexpr (n < N1) m_load(IC<1>{}, IC<14 - N1 + n>{}); else m_load(IC<2>{}, IC<n - N1>{});
      }
      SB();
      side(I);
      SB();
    });
  };

  if constexpr (DO_P3) {
    const int rg3 = 4 * (xcd >> 2) + (sl & 3), fb = 7 * (xcd & 3) + (sl >> 2);
    u32 mvo = (u32)((lane >> 4) & 1) * (4u * 56u * 1024u) + (u32)((2 * (w & 1) + (lane >> 5)) * 16 + t16) * 16u;
    OPQ(mvo);
    const u32 msw = WS_MBUF + (u32)(16 * rg3 * 56 + 2 * fb + (w >> 1)) * 1024u;
    auto m_store = [&](auto T, const u32x4& mo) {
      constexpr int t = decltype(T)::v;
      STG4(mo, mvo, srd_ws, msw + (u32)(((t & 1) * 8 + (t >> 1)) * 56) * 1024u);
    };
    const u32 f3o = WS_F3 + (u32)(rg3 * 2 * 32 + fb) * 32u + (u32)w * 8u;
    if (w != 0) {
      u32x4 sent = {~0u, ~0u, ~0u, ~0u};
      OPQ(sent);
      sfor<0, 8>([&](auto I) {
        constexpr int i = decltype(I)::v;
        STG4(sent, l16, srd_ws, WS_MBUF + (u32)((16 * rg3 + w + (i >> 1) * 4) * 56 + 2 * fb + (i & 1)) * 1024u);
      });
    }
    u32x4 csf;
    {
      float c = csa[0];
      u32 hi = pk2(c, c); float r1 = c - bflo(hi);
      u32 mi = pk2(r1, r1); float r2 = r1 - bflo(mi);
      u32 lo = pk2(r2, r2);
      csf = u32x4{hi, (hi & 0xffffu) | (mi & 0xffff0000u), (mi & 0xffffu) | (lo & 0xffff0000u), 0u};
      OPQa(csf);
    }
    TS(9);
    if (w == 0) {
      for (u32 it = 0; it < SPIN_MAX; ++it) {
        LDG2(fq, (u32)(lane & 31) * 8u, srd_ws, WS_F2 + (u32)rg3 * 256u);
        WAITv<0>(fq);
        if (f_ok()) break;
        __builtin_amdgcn_s_sleep(SLP);
      }
    }
    BAR();
    TS(8);
    const u32 hso = WS_HBUF + (u32)(rg3 * 8 * 32 + 7 * w) * 1024u, hst = WS_HBUF + (u32)(rg3 * 8 * 32 + 28 + w) * 1024u;
    auto h_m0 = [&](auto U, auto I, auto NOP) {
      constexpr int u = decltype(U)::v, i = decltype(I)::v;
      u32 v = i == 7 ? L_HT + (u32)((u & 3) * 4096) + (u32)w * 1024u : (u32)((u % 3) * 28672 + i * 1024) + (u32)w * 7168u;
      if constexpr (i == 0 || i == 4 || i == 7) { if constexpr (decltype(NOP)::v != 0) SETM0N(v); else SETM0(v); }
    };
    auto h_dma = [&](auto U, auto I) {
      constexpr int u = decltype(U)::v, i = decltype(I)::v;
      if constexpr (i == 7) DMAs<0>(l16, srd_ws, hst + (u32)u * 32768u);
      else DMAs<(i & 3) * 1024>(l16, srd_ws, hso + (u32)(u * 32768 + (i & 4) * 1024));
    };
    auto h_all = [&](auto U) { sfor<0, 8>([&](auto I) { h_m0(U, I, IC<1>{}); h_dma(U, I); }); };
    u32x4 stl[8];
    h_all(IC<0>{});
    {
      u32 sv = (u32)(lane & 31) * 16u + (u32)(lane >> 5) * 4096u;
      sfor<0, 8>([&](auto I) { LDGv<decltype(I)::v * 512>(stl[decltype(I)::v], sv, srd_ws, WS_HSTAT + (u32)((4 * rg3 + w) * 16 * 32) * 16u); });
    }
    h_all(IC<1>{});
    SB();

    f32x16 acc[2];
    u32x4 mo, ec, mfr;
    float ea[2], eb[2];
    auto epi = [&](auto T, auto E, auto Q) {
      constexpr int a = decltype(T)::v & 1, e = decltype(E)::v, q = decltype(Q)::v;
      const float g0 = acc[a][2 * e], g1 = acc[a][2 * e + 1];
      const float k1 = asf(ec.x), k2 = asf(ec.y), ev = asf(ec.z);
      if constexpr (q == 0) { ea[0] = g0 * g0; ea[1] = g1 * g1; ea[0] = __builtin_fmaf(ea[0], k2, k1); }
      if constexpr (q == 1) { ea[1] = __builtin_fmaf(ea[1], k2, k1); ea[0] *= g0; ea[1] *= g1; }
      if constexpr (q == 2) { ea[0] = __builtin_amdgcn_exp2f(ea[0]); eb[0] = g0 * acc[a][8 + 2 * e]; }
      if constexpr (q == 3) { ea[1] = __builtin_amdgcn_exp2f(ea[1]); eb[1] = g1 * acc[a][9 + 2 * e]; }
      if constexpr (q == 4) { ea[0] = __builtin_amdgcn_rcpf(__builtin_fmaf(ea[0], ev, ev)); }
      if constexpr (q == 5) { ea[1] = __builtin_amdgcn_rcpf(__builtin_fmaf(ea[1], ev, ev)); }
      if constexpr (q == 6) {
        mo[e] = pk2(eb[0] * ea[0], eb[1] * ea[1]);
        mo[e] = min(mo[e], 0xfffffffeu);
      }
      OPQ(ea[0]); OPQ(ea[1]); OPQ(eb[0]); OPQ(eb[1]);
    };
    ea[0] = ea[1] = eb[0] = eb[1] = 0.f;
    u32 hb0 = l16, hb1 = l16 + 57344u, stb = L_ST + (u32)(lane & 31) * 32u;
    OPQ(hb0); OPQ(hb1); OPQ(stb);
    auto hfrag = [&](auto GI) {
      constexpr int G = decltype(GI)::v, t = G >> 5, k = G & 31;
      constexpr u32 ad = k >= 28 ? L_HT + (t & 3) * 4096 + (k - 28) * 1024 : (t % 3) * 28672 + k * 1024;
      if constexpr (ad >= 57344u) return lds_r128(hb1 + (ad - 57344u)); else return lds_r128(hb0 + ad);
    };
    float sm, sq, mean;
    auto st_step = [&](auto I) {
      constexpr int i = decltype(I)::v;
      if constexpr (i < 4) {
        f32x4 v = __builtin_bit_cast(f32x4, stl[2 * i]) + __builtin_bit_cast(f32x4, stl[2 * i + 1]);
        stl[2 * i] = __builtin_bit_cast(u32x4, v); OPQ(stl[2 * i]);
      }
      if constexpr (i == 4 || i == 5) {
        f32x4 v = __builtin_bit_cast(f32x4, stl[4 * (i - 4)]) + __builtin_bit_cast(f32x4, stl[4 * (i - 4) + 2]);
        stl[4 * (i - 4)] = __builtin_bit_cast(u32x4, v); OPQ(stl[4 * (i - 4)]);
      }
      if constexpr (i == 6) {
        f32x4 v = __builtin_bit_cast(f32x4, stl[0]) + __builtin_bit_cast(f32x4, stl[4]);
        stl[0] = __builtin_bit_cast(u32x4, v); OPQ(stl[0]);
      }
      if constexpr (i == 7) {
        auto a = __builtin_amdgcn_permlane32_swap(stl[0].x, stl[0].z, false, false);
        auto c = __builtin_amdgcn_permlane32_swap(stl[0].y, stl[0].w, false, false);
        sm = asf(a[0]) + asf(a[1]); sq = asf(c[0]) + asf(c[1]);
        OPQ(sm); OPQ(sq);
      }
      if constexpr (i == 8) {
        mean = sm * (1.f / 512.f);
        sq = fmaxf(__builtin_fmaf(-mean, mean, sq * (1.f / 512.f)), 0.f) + 1e-5f;
        OPQ(mean); OPQ(sq);
      }
      if constexpr (i == 9) {
        float rstd = __builtin_amdgcn_rsqf(sq);
        const float C1 = -2.f * 0.7978845608028654f * 1.4426950408889634f;
        sm = C1 * rstd;
        stl[1] = u32x4{asu(sm), asu(rstd * rstd), asu(sq), 0u};
        OPQ(stl[1]);
      }
      if constexpr (i == 10) {
        stl[1].y = asu(asf(stl[1].y) * asf(stl[1].x) * 0.044715f);
        float m = -mean;
        u32 hi = pk2(m, m); float r1 = m - bflo(hi);
        stl[2].x = hi; sm = r1;
        OPQ(stl[1]); OPQ(stl[2]); OPQ(sm);
      }
      if constexpr (i == 11) {
        u32 mi = pk2(sm, sm); float r2 = sm - bflo(mi);
        stl[2].y = mi; stl[2].z = pk2(r2, r2);
        OPQ(stl[2]);
      }
      if constexpr (i == 12) {
        u32 hi = stl[2].x, mi = stl[2].y, lo = stl[2].z;
        stl[3] = u32x4{(hi & 0xffffu) | (mi & 0xffff0000u), (lo & 0xffffu) | (hi & 0xffff0000u), (mi & 0xffffu) | (hi & 0xffff0000u), 0u};
        OPQ(stl[3]);
      }
      if constexpr (i == 13) {
        u32 a = L_ST + (u32)((2 * q4 + (w >> 1)) * 32 + (w & 1) * 16 + t16) * 32u;
        lds_w128(a, stl[1]);
        lds_w128(a + 16u, stl[3]);
      }
    };
    constexpr int WD = 4;
    u32x4 hf[WD + 1];
    BAR_VM(16);
    TS(47);
    sfor<0, WD>([&](auto I) { hf[decltype(I)::v] = hfrag(I); });
    sfor<0, 256>([&](auto GI) {
      constexpr int G = decltype(GI)::v, t = G >> 5, k = G & 31, a = t & 1;
      constexpr int ek = P3K[G], eA = P3A[G], eB = P3B[G], rk = P3RK[G], rA = P3RA[G], rB = P3RB[G];
      if constexpr (k == 28 && t < 7) {
        if constexpr (t == 0) BAR_VM(p3_after(E_H, 1, G));
        else asm volatile("s_waitcnt vmcnt(%0)\n\ts_barrier" ::"n"(p3_after(E_H, t + 1, G)) : "memory");
      }
      if constexpr (ek == E_H) h_m0(IC<eA>{}, IC<eB>{}, IC<0>{});
      if constexpr (ek == E_D && eB == 0) d_m0(IC<eA>{}, IC<0>{});
      if constexpr (k == 0) MMA32W_INIT(acc[a], Wf[k], hf[G % (WD + 1)]);
      else MMA32W_ACC(acc[a], Wf[k], hf[G % (WD + 1)]);
      SB();
      if constexpr (G + WD < 256) hf[(G + WD) % (WD + 1)] = hfrag(IC<G + WD>{});
      OPQ(hf[G % (WD + 1)]);
      SB();
      if constexpr (k == 31) MMA32W_ACC(acc[a], csf, mfr);
      static_assert(!(ek != 0 && rk != 0), "");
      if constexpr (ek == E_H) h_dma(IC<eA>{}, IC<eB>{});
      if constexpr (ek == E_D) d_dma(IC<eA>{}, IC<eB>{});
      if constexpr (rk == E_R) {
        if constexpr (rB == 0 && rA >= 3) WAIT_VM(p3_after(E_D, rA, G));
        d_rd(IC<rA>{}, IC<rB>{});
        if constexpr (rA >= 1 && rA <= B4A && (rB & 1)) OPQa(B4[rA - 1][rB >> 1]);
      }
      if constexpr (ek == E_T) touchD(IC<TJ2>{}, IC<14>{});
      if constexpr (ek == E_M) m_load(IC<eA / 14>{}, IC<eA % 14>{});
      if constexpr (ek == E_FL) LDG2(fq, f3v, srd_ws, f3p);
      if constexpr (rk == E_FC) { WAITv<p3_after(E_FL, rA, G)>(fq); f_spin(f3p, IC<SLP>{}); }
      if constexpr (ek == E_FS) {
        WAIT_VM(p3_after(E_ST, eA, G));
        static_assert(G > p3_last(E_ST, eA), "");
        STF(tag2, srd_ws, f3o + (u32)eA * 1024u);
      }
      if constexpr (ek == E_ST) {
        static_assert(G >= 32 * eA + 63 && G <= 32 * eA + 72, "");
        m_store(IC<eA>{}, mo);
      }
      if constexpr (k == (t == 0 ? 28 : 20)) mfr = lds_r128(stb + t * 1024 + 16);
      if constexpr (k == 1 && t > 0) ec = lds_r128(stb + (t - 1) * 1024);
      if constexpr (G == 255) ec = lds_r128(stb + 7 * 1024);
      SB();
      if constexpr (t > 0) {
        if constexpr (k >= 3 && k < 31) epi(IC<t - 1>{}, IC<(k - 3) / 7>{}, IC<(k - 3) % 7>{});
      } else {
        if constexpr (k == 12) {
          WAITv<8 + p3_cum(13)>(stl[0]);
          sfor<1, 8>([&](auto I) { TIE(stl[decltype(I)::v]); });
        }
        if constexpr (k >= 12 && k < 26) st_step(IC<k - 12>{});
      }
      if constexpr (k == 31) TS(24 + t);
      SB();
    });
    TS(45);
    WAIT_VM(p3_after(E_M, 13, 256));
    asm volatile("" ::"v"(tv));
    piece0([&](auto I) {
      constexpr int i = decltype(I)::v;
      if constexpr (i == 0) asm volatile("s_nop 7");
      if constexpr (i >= 2 && i < 16) {
        epi(IC<7>{}, IC<(2 * i - 4) / 7>{}, IC<(2 * i - 4) % 7>{});
        epi(IC<7>{}, IC<(2 * i - 3) / 7>{}, IC<(2 * i - 3) % 7>{});
      }
      if constexpr (i == SP) m_store(IC<7>{}, mo);
    });
  } else {
    touchD(IC<TJ2>{}, IC<14>{});
    sfor<0, 3>([&](auto R) {
      constexpr int r = decltype(R)::v;
      WAIT_VM(0);
      sfor<0, 3>([&](auto J) { d_rd8(IC<3 * r + decltype(J)::v>{}); });
      WAIT_LGKM0();
      sfor<0, 12>([&](auto I) { OPQa(B4[3 * r + decltype(I)::v / 4][decltype(I)::v & 3]); });
      sfor<0, 3>([&](auto J) { d_all(IC<3 * r + 3 + decltype(J)::v>{}); });
      SB();
    });
    d_all(IC<12>{}); d_all(IC<13>{});
    if (sl == 31 && xcd == 7) {
#pragma unroll 1
      for (int i = 0; i < 4; i++) {
        u32 o = WS_F1 + (u32)((w * 4 + i) * 64 + lane) * 8u;
        for (u32 it = 0; it < SPIN_MAX; ++it) {
          u64 f = ld_flag(A.ws, o);
          if (__all(f == tag)) break;
          __builtin_amdgcn_s_sleep(8);
        }
      }
      BAR();
      if (w == 0 && lane == 0) st_flag(A.ws, WS_EPOCH, tag);
    }
    fq = u32x2{~tag2.x, 0u};
    f_spin(f3p, IC<16>{});
    sfor<0, 14>([&](auto J) { m_load(IC<0>{}, J); });
    WAIT_VM(0);
    SB();
    asm volatile("" ::"v"(tv));
    piece0([&](auto) {});
  }

  TS(11);
  auto put = [&](auto Q) {
    constexpr int q = decltype(Q)::v;
    const u32 a = (q < 2 ? dsb : dsx) + (u32)(q & 1) * 4096u + l16;
#pragma unroll
    for (int c = 0; c < 4; c++) lds_w128(a + c * 1024, __builtin_bit_cast(u32x4, pc[q & 1][c]));
  };
  f32x4 fp[2][4];
  u32x2 o2[2];
  auto fin = [&](auto S, auto I) {
    constexpr int s = decltype(S)::v, i = decltype(I)::v;
    constexpr u32 str = s == 0 ? 12288u : 8192u;
    if constexpr (i < 2) {
      u32 a = (s == 0 ? L_DS : L_DX) + (u32)utt * 4096u + (u32)uG * 2048u + l16;
#pragma unroll
      for (int v = 0; v < 4; v++) fp[i][v] = __builtin_bit_cast(f32x4, lds_r128(a + i * 1024 + v * str));
    } else if constexpr (i < 4) {
      constexpr int c = i - 2;
      u32 ha = hres[s][2 * c], hb = hres[s][2 * c + 1];
      float r[4] = {bflo(ha), bfhi(ha), bflo(hb), bfhi(hb)};
#pragma unroll
      for (int e = 0; e < 4; e++) {
        float u = fp[c][0][e] + fp[c][1][e], v = fp[c][2][e] + fp[c][3][e];
        OPQ(u); OPQ(v);
        u += v; OPQ(u);
        r[e] += u; OPQ(r[e]);
      }
      o2[c] = u32x2{pk2(r[0], r[1]), pk2(r[2], r[3])};
    } else {
      auto sx = __builtin_amdgcn_permlane16_swap(o2[0].x, o2[1].x, false, false);
      auto sy = __builtin_amdgcn_permlane16_swap(o2[0].y, o2[1].y, false, false);
      u32x4 ov = {sx[0], sy[0], sx[1], sy[1]};
      __builtin_amdgcn_raw_buffer_store_b128(ov, rs_out, (u32)t16 * 1024u + (u32)((q4 & 1) * 32 + (q4 >> 1) * 16),
                                             (u32)((64 * rt + 32 * s + 16 * utt) * 1024 + ct * 128 + uG * 64), 0);
    }
  };
  auto retry = [&](auto Q) {
    constexpr int q = decltype(Q)::v;
    if (__builtin_expect(__any(pc[q & 1][0][0] != pc[q & 1][0][0]), 0)) {
#pragma unroll 1
      for (u32 it = 0; it < SPIN_MAX; ++it) {
        u32 mx = 0;
#pragma unroll
        for (int j = 0; j < 14; j++) mx = max(max(max(mf[q][j].x, mf[q][j].y), max(mf[q][j].z, mf[q][j].w)), mx);
        if (!__any(mx == ~0u)) break;
        __builtin_amdgcn_s_sleep(DO_P3 || q == 3 ? 1 : 12);
        sfor<0, 14>([&](auto J) { m_load(Q, J); });
        sfor<0, 14>([&](auto J) {
          constexpr int j = decltype(J)::v;
          if constexpr (DO_P3 && (q == 1 || q == 2)) WAITa<13 - j>(mf[q][j]); else WAITv<13 - j>(mf[q][j]);
          kstep(Q, J);
          SB();
        });
        if (!__any(pc[q & 1][0][0] != pc[q & 1][0][0])) break;
      }
    }
    put(Q);
  };
  constexpr int N2 = NV - N1;
  if constexpr (!DO_P3) retry(IC<0>{});
  WAIT_VM(DO_P3 ? N2 + (3 * (N1 - 1) + 1 < SP ? 1 : 0) : 0);
  mtie(IC<1>{});
  sfor<0, 14>([&](auto J) {
    constexpr int j = decltype(J)::v;
    kstep(IC<1>{}, J);
    SB();
    if constexpr (DO_P3 && j == 1) put(IC<0>{});
    if constexpr (N2 + j < 14) m_load(IC<2>{}, IC<N2 + j>{});
    SB();
  });
  TS(12);
  if constexpr (!DO_P3) retry(IC<1>{});
  WAIT_VM(0);
  mtie(IC<2>{});
  sfor<0, 14>([&](auto J) {
    constexpr int j = decltype(J)::v;
    kstep(IC<2>{}, J);
    if constexpr (j == 4 || j == 5) fin(IC<0>{}, IC<j - 4>{});
    if constexpr (j >= 7 && j < 10) fin(IC<0>{}, IC<j - 5>{});
    if constexpr (j >= 4 && j < 10) {
#pragma unroll
      for (int i = 0; i < 4; i++) {
        __builtin_amdgcn_sched_group_barrier(0x008, 1, 0);
        __builtin_amdgcn_sched_group_barrier(0x100, 1, 0);
        __builtin_amdgcn_sched_group_barrier(0x002, 4, 0);
      }
    }
    SB();
    if constexpr (DO_P3 && j == 1) retry(IC<1>{});
    if constexpr (j == 3) BAR();
    if constexpr (M3D == 0) m_load(IC<3>{}, J);
    if constexpr (M3D == 1 && j >= 7) { m_load(IC<3>{}, IC<2 * (j - 7)>{}); m_load(IC<3>{}, IC<2 * (j - 7) + 1>{}); }
    SB();
  });
  TS(13);
  if constexpr (!DO_P3) retry(IC<2>{});
  sfor<0, 14>([&](auto J) {
    constexpr int j = decltype(J)::v;
    WAITv<13 - j + (j < (M3D ? 4 : 9) ? 1 : 0)>(mf[3][j]);
    kstep(IC<3>{}, J);
    SB();
    if constexpr (DO_P3 && j == 1) retry(IC<2>{});
    SB();
  });
  retry(IC<3>{});
  BAR();
  sfor<0, 5>([&](auto I) { fin(IC<1>{}, I); });
  TS(14);
}

extern "C" void kernel_launch(void* const* d_in, const int* in_sizes, int n_in, void* d_out, int out_size,
                              void* d_ws, size_t ws_size, hipStream_t stream) {
  Args a;
  a.x = (const char*)d_in[0]; a.wqkv = (const char*)d_in[1]; a.wo = (const char*)d_in[2];
  a.wg = (const char*)d_in[4]; a.wu = (const char*)d_in[5]; a.wd = (const char*)d_in[6];
  a.out = (char*)d_out; a.ws = (char*)d_ws;
  fused_block_kernel<<<256, 256, 0, stream>>>(a);
}
```
